```python
import torch

def _softmax(x, axis=-1):
    return torch.softmax(x, dim=axis)
def _gelu(x):
    return torch.nn.functional.gelu(x, approximate="tanh")
def _silu(x):
    return torch.nn.functional.silu(x)
def _sigmoid(x):
    return torch.sigmoid(x)
def _layernorm(x, axis=-1, eps=1e-5):
    m = x.mean(dim=axis, keepdim=True)
    v = x.var(dim=axis, keepdim=True, unbiased=False)
    return (x - m) / torch.sqrt(v + eps)
def _scatter_add(tgt, idx, src, axis=0):
    return tgt.clone().index_add_(axis, idx, src)
def _segment_sum(src, seg, n):
    out = torch.zeros((n,) + src.shape[1:], dtype=src.dtype)
    return out.index_add_(0, seg, src)
def _pad(x, pw):
    flat = tuple(v for pair in reversed(pw) for v in pair)
    return torch.nn.functional.pad(x, flat)

kernel_name = 'gated_mix__sliding_block__dense_activation_relu__prompt'

def setup_inputs():

    _w = (lambda a: (lambda t: t.to(torch.bfloat16) if t.is_floating_point() else t)(torch.as_tensor(a)))
    def _A_setup():
        B, S, D = 1, 2048, 448
        return {
            "x": _w((lambda *s: torch.randn(*s, dtype=torch.bfloat16) * 0.05)(B, S, D)),
            "wqkv": _w((lambda *s: torch.randn(*s, dtype=torch.bfloat16) * 1.6)(D, 3 * D)),
            "wo": _w((lambda *s: torch.randn(*s, dtype=torch.bfloat16) * 0.05)(D, D)),
        }
    _A_in = _A_setup()

    def _B_setup():
        B, S, D, Dff = 1, 2048, 448, 1792
        return {
            "x": _w((lambda *s: torch.randn(*s, dtype=torch.bfloat16) * 0.05)(B, S, D)),
            "w1": _w((lambda *s: torch.randn(*s, dtype=torch.bfloat16) * 0.2)(D, Dff)),
            "w2": _w((lambda *s: torch.randn(*s, dtype=torch.bfloat16) * 0.2)(Dff, D)),
        }
    _B_in = _B_setup()

    return {
        "A_x": _A_in["x"],
        "A_wqkv": _A_in["wqkv"],
        "A_wo": _A_in["wo"],
        "B_x": _B_in["x"],
        "B_w1": _B_in["w1"],
        "B_w2": _B_in["w2"],
        "gate_w": _w((lambda *s: torch.randn(*s, dtype=torch.bfloat16) * 0.05)(448) * 16.0),
    }

def reference(A_x, A_wqkv, A_wo, B_x, B_w1, B_w2, gate_w):
    def _A_ref(x, wqkv, wo):
        B, S, D = x.shape
        H = 16; Dh = D // H; W = 256
        qkv = (lambda x, *d: x.permute(*d))((x @ wqkv).reshape(B, S, 3, H, Dh), 2, 0, 3, 1, 4)
        q, k, v = qkv[0], qkv[1], qkv[2]
        scores = torch.einsum("bhqd,bhkd->bhqk", q, k) * (Dh ** -0.5)
        idx = torch.arange(S)
        mask = (idx[:, None] - idx[None, :] >= 0) & (idx[:, None] - idx[None, :] < W)
        scores = torch.where(mask[None, None], scores, -1e9)
        p = _softmax(scores, axis=-1)
        o = torch.einsum("bhqk,bhkd->bhqd", p, v)
        return (lambda x, *d: x.permute(*d))(o, 0, 2, 1, 3).reshape(B, S, D) @ wo

    def _B_ref(x, w1, w2):
        h = torch.einsum("bsd,df->bsf", x, w1)
        zero = torch.zeros_like(h)
        h = torch.maximum(h, zero)
        return torch.einsum("bsf,fd->bsd", h, w2)

    x = A_x
    g = _sigmoid((x * gate_w).sum(-1, keepdims=True))
    a = _A_ref(x, A_wqkv, A_wo)
    b = _B_ref(x, B_w1, B_w2)
    return g * a + (1 - g) * b
```

```cpp
#include <hip/hip_runtime.h>
#include <hip/hip_bf16.h>
typedef __hip_bfloat16 bf16;
constexpr int S=2048, D=448, H=16, DH=28, W=256, DFF=1792;

__device__ inline float b2f(bf16 x){ return __bfloat162float(x);}
__device__ inline bf16 f2b(float x){ return __float2bfloat16(x);}

__global__ void gemm_naive(const bf16* A, const bf16* B, bf16* C, int M, int N, int K, int relu){
  int n = blockIdx.x*64 + threadIdx.x; int m = blockIdx.y*4 + threadIdx.y;
  if(n>=N) return;
  float acc=0;
  for(int k=0;k<K;k++) acc += b2f(A[m*K+k])*b2f(B[k*N+n]);
  if(relu) acc = fmaxf(acc,0.f);
  C[m*N+n]=f2b(acc);
}
__global__ void attn_naive(const bf16* qkv, bf16* o){
  int q = blockIdx.x*64+threadIdx.x; int h = blockIdx.y;
  float qv[DH];
  #pragma unroll
  for(int d=0;d<DH;d++) qv[d]=b2f(qkv[q*3*D + h*DH + d]);
  float m=-1e30f;
  int k0 = q-W+1; if(k0<0) k0=0;
  float sc = rsqrtf((float)DH);
  for(int k=k0;k<=q;k++){
    float s=0;
    #pragma unroll
    for(int d=0;d<DH;d++) s+=qv[d]*b2f(qkv[k*3*D + D + h*DH + d]);
    m=fmaxf(m,s*sc);
  }
  float l=0; float acc[DH];
  #pragma unroll
  for(int d=0;d<DH;d++) acc[d]=0;
  for(int k=k0;k<=q;k++){
    float s=0;
    #pragma unroll
    for(int d=0;d<DH;d++) s+=qv[d]*b2f(qkv[k*3*D + D + h*DH + d]);
    float p=__expf(s*sc-m); l+=p;
    #pragma unroll
    for(int d=0;d<DH;d++) acc[d]+=p*b2f(qkv[k*3*D + 2*D + h*DH + d]);
  }
  #pragma unroll
  for(int d=0;d<DH;d++) o[q*D + h*DH + d]=f2b(acc[d]/l);
}
__global__ void combine(const bf16* x, const bf16* gw, const bf16* a, const bf16* b, bf16* out){
  int m = blockIdx.x; int t = threadIdx.x;
  __shared__ float red[64];
  float s=0;
  for(int d=t; d<D; d+=64) s += b2f(f2b(b2f(x[m*D+d])*b2f(gw[d])));
  red[t]=s; __syncthreads();
  if(t==0){ float tot=0; for(int i=0;i<64;i++) tot+=red[i]; red[0]=tot;}
  __syncthreads();
  float z = b2f(f2b(red[0]));
  float g = b2f(f2b(1.f/(1.f+__expf(-z))));
  float og = b2f(f2b(1.f-g));
  for(int d=t; d<D; d+=64) out[m*D+d]=f2b(b2f(f2b(g*b2f(a[m*D+d]))) + b2f(f2b(og*b2f(b[m*D+d]))));
}

extern "C" void kernel_launch(void* const* d_in, const int* in_sizes, int n_in,
                              void* d_out, int out_size, void* d_ws, size_t ws_size,
                              hipStream_t stream) {
  const bf16* x=(const bf16*)d_in[0]; const bf16* wqkv=(const bf16*)d_in[1]; const bf16* wo=(const bf16*)d_in[2];
  const bf16* w1=(const bf16*)d_in[4]; const bf16* w2=(const bf16*)d_in[5]; const bf16* gw=(const bf16*)d_in[6];
  bf16* qkv=(bf16*)d_ws; bf16* o=qkv+S*3*D; bf16* a=o+S*D; bf16* h=a+S*D; bf16* b=h+S*DFF;
  gemm_naive<<<dim3(3*D/64,S/4),dim3(64,4),0,stream>>>(x,wqkv,qkv,S,3*D,D,0);
  attn_naive<<<dim3(S/64,H),64,0,stream>>>(qkv,o);
  gemm_naive<<<dim3(D/64,S/4),dim3(64,4),0,stream>>>(o,wo,a,S,D,D,0);
  gemm_naive<<<dim3(DFF/64,S/4),dim3(64,4),0,stream>>>(x,w1,h,S,DFF,D,1);
  gemm_naive<<<dim3(D/64,S/4),dim3(64,4),0,stream>>>(h,w2,b,S,D,DFF,0);
  combine<<<S,64,0,stream>>>(x,gw,a,b,(bf16*)d_out);
}
```

```cpp
#include <hip/hip_runtime.h>
#include <stdint.h>

typedef __attribute__((ext_vector_type(8))) __bf16 bf16x8;
typedef __attribute__((ext_vector_type(2))) __bf16 bf16x2;
typedef __attribute__((ext_vector_type(2))) float f32x2;
typedef __attribute__((ext_vector_type(4))) float f32x4;
typedef __attribute__((ext_vector_type(4))) short s16x4;
typedef __attribute__((ext_vector_type(4))) unsigned u32x4;
typedef __attribute__((ext_vector_type(2))) unsigned u32x2;
typedef __attribute__((address_space(3))) void* lds_vptr;
typedef __attribute__((address_space(3))) s16x4* lds_s16x4p;
typedef __attribute__((address_space(3))) u32x4* lds_u32x4p;
typedef __attribute__((address_space(3))) u32x2* lds_u32x2p;
typedef __attribute__((address_space(3))) f32x4* lds_f32x4p;
#define DI __device__ __forceinline__

constexpr int S_ = 2048, D_ = 448, NH = 16, DH = 28, FF = 1792;
constexpr unsigned OFF_Q = 0u, OFF_K = 2u << 20, OFF_V = 4u << 20, OFF_O = 6u << 20, OFF_H = 8u << 20, OFF_G = 16u << 20;
constexpr unsigned WS_BYTES = 17u << 20;

DI unsigned pk2(float a, float b) {
  f32x2 f = {a, b};
  return __builtin_bit_cast(unsigned, __builtin_convertvector(f, bf16x2));
}
DI float bf2f(unsigned short v) { return __uint_as_float(((unsigned)v) << 16); }
DI float rbf(float x) { return __uint_as_float(pk2(x, 0.f) << 16); }
DI f32x4 mfma16(bf16x8 a, bf16x8 b, f32x4 c) { return __builtin_amdgcn_mfma_f32_16x16x32_bf16(a, b, c, 0, 0, 0); }
DI bf16x8 lds_rd128(unsigned addr) { return __builtin_bit_cast(bf16x8, *(lds_u32x4p)(uintptr_t)addr); }
DI bf16x8 lds_rdtr(unsigned a0, unsigned a1) {
  s16x4 lo = __builtin_amdgcn_ds_read_tr16_b64_v4i16((lds_s16x4p)(uintptr_t)a0);
  s16x4 hi = __builtin_amdgcn_ds_read_tr16_b64_v4i16((lds_s16x4p)(uintptr_t)a1);
  return __builtin_bit_cast(bf16x8, __builtin_shufflevector(lo, hi, 0, 1, 2, 3, 4, 5, 6, 7));
}
DI __amdgpu_buffer_rsrc_t mkrsrc(const void* p, unsigned bytes) { return __builtin_amdgcn_make_buffer_rsrc((void*)p, 0, bytes, 0x00020000); }
DI void glds16(__amdgpu_buffer_rsrc_t rs, unsigned lds_addr, unsigned voff, unsigned soff) {
  __builtin_amdgcn_raw_ptr_buffer_load_lds(rs, (lds_vptr)(uintptr_t)lds_addr, 16, voff, soff, 0, 0);
}
#define WAITV(n) asm volatile("s_waitcnt vmcnt(" #n ")" ::: "memory")
#define WAITL0() asm volatile("s_waitcnt lgkmcnt(0)" ::: "memory")
#define BAR() __builtin_amdgcn_s_barrier()
#define SCHED0() __builtin_amdgcn_sched_barrier(0)

constexpr int K1_XB = 16384, K1_WB = 28672, K1_STAGE = K1_XB + K1_WB, K1_LDS = 3 * K1_STAGE;

template <bool ISV>
DI void k1_mainloop(f32x4 (&acc)[7][4], __amdgpu_buffer_rsrc_t rx, __amdgpu_buffer_rsrc_t rw, const unsigned (&vx)[2],
                    const unsigned (&vw)[7], unsigned sx0, unsigned sw0, unsigned wstep, int w, unsigned lds0,
                    unsigned xa0, unsigned xa1, unsigned wa_e, unsigned wa_o) {
  auto issue = [&](int s, int buf) {
    unsigned lb = lds0 + buf * K1_STAGE;
#pragma unroll
    for (int u = 0; u < 4; ++u) glds16(rx, lb + (4 * w + u) * 1024, vx[u & 1], sx0 + s * 128 + u * (8 * 896));
#pragma unroll
    for (int u = 0; u < 7; ++u) glds16(rw, lb + K1_XB + (7 * w + u) * 1024, vw[u], sw0 + s * wstep);
  };
  issue(0, 0); issue(1, 1); issue(2, 2);
  bf16x8 xf[2][4], wf[2][7];
  auto rd = [&](int buf, int ks, bf16x8 (&X)[4], bf16x8 (&Wf)[7]) {
    unsigned bo = buf * K1_STAGE;
    unsigned xa = (ks ? xa1 : xa0) + bo;
#pragma unroll
    for (int mt = 0; mt < 4; ++mt) X[mt] = lds_rd128(xa + mt * 2048);
    unsigned we = wa_e + bo + ks * (32 * 448), wo_ = wa_o + bo + ks * (32 * 448);
#pragma unroll
    for (int c = 0; c < 7; ++c) {
      unsigned a = ((c & 1) ? wo_ : we) + c * 32;
      Wf[c] = lds_rdtr(a, a + 4 * 448);
    }
  };
  auto mm = [&](bf16x8 (&X)[4], bf16x8 (&Wf)[7]) {
#pragma unroll
    for (int c = 0; c < 7; ++c)
#pragma unroll
      for (int mt = 0; mt < 4; ++mt)
        acc[c][mt] = ISV ? mfma16(X[mt], Wf[c], acc[c][mt]) : mfma16(Wf[c], X[mt], acc[c][mt]);
  };
  WAITV(22); BAR();
  rd(0, 0, xf[0], wf[0]);
  int buf = 0;
  for (int s = 0; s < 7; ++s) {
    rd(buf, 1, xf[1], wf[1]);
    mm(xf[0], wf[0]);
    SCHED0();
    int nb = buf == 2 ? 0 : buf + 1;
    if (s < 6) {
      if (s < 5) { asm volatile("s_waitcnt vmcnt(11) lgkmcnt(0)" ::: "memory"); }
      else { asm volatile("s_waitcnt vmcnt(0) lgkmcnt(0)" ::: "memory"); }
      BAR();
      if (s + 3 < 7) issue(s + 3, buf);
      rd(nb, 0, xf[0], wf[0]);
    }
    SCHED0();
    mm(xf[1], wf[1]);
    buf = nb;
  }
}

__global__ __launch_bounds__(256, 1) void k1_gemm(const unsigned short* __restrict__ x, const unsigned short* __restrict__ wqkv,
                                                  const unsigned short* __restrict__ w1, const unsigned short* __restrict__ gw,
                                                  char* __restrict__ ws) {
  extern __shared__ __attribute__((aligned(16))) char smem[];
  const int tid = threadIdx.x, lane = tid & 63;
  const int w = __builtin_amdgcn_readfirstlane(tid >> 6);
  const int xcd = blockIdx.x & 7, idx = blockIdx.x >> 3;
  if (idx >= 28) {
    const int t0 = (xcd * 4 + (idx - 28)) * 64 + w * 16;
    u32x4 gv = {0, 0, 0, 0};
    if (lane < 56) gv = *(const u32x4*)(gw + lane * 8);
    float* g = (float*)(ws + OFF_G);
    for (int t = 0; t < 16; ++t) {
      u32x4 xv = {0, 0, 0, 0};
      if (lane < 56) xv = *(const u32x4*)(x + (size_t)(t0 + t) * D_ + lane * 8);
      float s = 0.f;
#pragma unroll
      for (int j = 0; j < 4; ++j) {
        s += rbf(__uint_as_float(xv[j] << 16) * __uint_as_float(gv[j] << 16));
        s += rbf(__uint_as_float(xv[j] & 0xffff0000u) * __uint_as_float(gv[j] & 0xffff0000u));
      }
#pragma unroll
      for (int o = 32; o > 0; o >>= 1) s += __shfl_xor(s, o);
      float z = rbf(s);
      float gg = rbf(1.f / (1.f + __expf(-z)));
      if (lane == 0) g[t0 + t] = gg;
    }
    return;
  }
  const int tt = 2 * xcd + (idx >= 14 ? 1 : 0), ct = idx >= 14 ? idx - 14 : idx;
  const int wm = w & 1, wn = w >> 1;
  const bool isq = ct < 6;
  const unsigned strideB = isq ? 2688u : 3584u;
  const unsigned col0B = (isq ? ct : ct - 6) * 448u;
  __amdgpu_buffer_rsrc_t rx = mkrsrc(x, S_ * D_ * 2);
  __amdgpu_buffer_rsrc_t rw = mkrsrc(isq ? wqkv : w1, isq ? D_ * 3 * D_ * 2 : D_ * FF * 2);
  unsigned vx[2], vw[7];
#pragma unroll
  for (int u = 0; u < 2; ++u) {
    int r = lane >> 3;
    int sw = ((lane >> 4) + 4 * u) & 7;
    vx[u] = r * 896 + (((lane & 7) ^ sw) << 4);
  }
#pragma unroll
  for (int u = 0; u < 7; ++u) {
    int L = 64 * (7 * w + u) + lane;
    int row = L / 28, p = L - row * 28;
    int cc = p ^ (((row >> 3) & 1) << 1);
    vw[u] = row * strideB + cc * 16;
  }
  const unsigned sx0 = (unsigned)(128 * tt + 32 * w) * 896u;
  const unsigned lds0 = (unsigned)(uintptr_t)(lds_vptr)smem;
  const int i16 = lane & 15, g = lane >> 4;
  const unsigned xrow = (64 * wm + i16) * 128;
  const unsigned xa0 = lds0 + xrow + (((g) ^ (i16 >> 1)) << 4);
  const unsigned xa1 = lds0 + xrow + (((4 + g) ^ (i16 >> 1)) << 4);
  const unsigned wbase = lds0 + K1_XB + (8 * g + (i16 >> 2)) * 448 + 8 * (i16 & 3) + 7 * wn * 32;
  const int b = g & 1;
  const unsigned wa_e = wbase + ((wn & 1) ? -32 * b : 32 * b);
  const unsigned wa_o = wbase + ((wn & 1) ? 32 * b : -32 * b);
  f32x4 acc[7][4];
#pragma unroll
  for (int c = 0; c < 7; ++c)
#pragma unroll
    for (int m = 0; m < 4; ++m) acc[c][m] = f32x4{0.f, 0.f, 0.f, 0.f};
  const bool isv = (ct == 4 || ct == 5);
  if (isv) k1_mainloop<true>(acc, rx, rw, vx, vw, sx0, col0B, 64 * strideB, w, lds0, xa0, xa1, wa_e, wa_o);
  else k1_mainloop<false>(acc, rx, rw, vx, vw, sx0, col0B, 64 * strideB, w, lds0, xa0, xa1, wa_e, wa_o);

  const int t16 = 8 * tt + 4 * wm;
  if (ct >= 6) {
    char* hb = ws + OFF_H + lane * 16;
    const int m0 = 7 * (ct - 6);
#pragma unroll
    for (int mt = 0; mt < 4; ++mt) {
      unsigned p[7][2];
#pragma unroll
      for (int c = 0; c < 7; ++c) {
        p[c][0] = pk2(fmaxf(acc[c][mt][0], 0.f), fmaxf(acc[c][mt][1], 0.f));
        p[c][1] = pk2(fmaxf(acc[c][mt][2], 0.f), fmaxf(acc[c][mt][3], 0.f));
      }
      char* hp = hb + (size_t)((t16 + mt) * 56 + m0) * 1024;
      if (wn == 0) {
#pragma unroll
        for (int q = 0; q < 3; ++q) *(u32x4*)(hp + q * 1024) = u32x4{p[2 * q][0], p[2 * q][1], p[2 * q + 1][0], p[2 * q + 1][1]};
        *(u32x2*)(hp + 3 * 1024) = u32x2{p[6][0], p[6][1]};
      } else {
        *(u32x2*)(hp + 3 * 1024 + 8) = u32x2{p[0][0], p[0][1]};
#pragma unroll
        for (int q = 0; q < 3; ++q) *(u32x4*)(hp + (4 + q) * 1024) = u32x4{p[2 * q + 1][0], p[2 * q + 1][1], p[2 * q + 2][0], p[2 * q + 2][1]};
      }
    }
  } else if (!isv) {
    const int h0 = (ct & 1) * 8 + wn * 4;
    char* qb = ws + (ct < 2 ? OFF_Q : OFF_K) + lane * 16;
#pragma unroll
    for (int mt = 0; mt < 4; ++mt) {
      unsigned p[7][2];
#pragma unroll
      for (int c = 0; c < 7; ++c) {
        p[c][0] = pk2(acc[c][mt][0], acc[c][mt][1]);
        p[c][1] = pk2(acc[c][mt][2], acc[c][mt][3]);
      }
      u32x4 f0 = {p[0][0], p[0][1], g < 3 ? p[1][0] : 0u, g < 3 ? p[1][1] : 0u};
      u32x4 f1 = {p[2][0], p[2][1], g < 2 ? p[3][0] : (g == 2 ? 0u : p[1][0]), g < 2 ? p[3][1] : (g == 2 ? 0u : p[1][1])};
      u32x4 f2 = {p[4][0], p[4][1], g == 0 ? p[5][0] : (g == 1 ? 0u : p[3][0]), g == 0 ? p[5][1] : (g == 1 ? 0u : p[3][1])};
      u32x4 f3 = {p[6][0], p[6][1], g == 0 ? 0u : p[5][0], g == 0 ? 0u : p[5][1]};
      char* qp = qb + (size_t)(h0 * 128 + t16 + mt) * 1024;
      *(u32x4*)(qp) = f0;
      *(u32x4*)(qp + 128 * 1024) = f1;
      *(u32x4*)(qp + 2 * 128 * 1024) = f2;
      *(u32x4*)(qp + 3 * 128 * 1024) = f3;
    }
  } else {
    const int h0 = (ct & 1) * 8 + wn * 4;
    const int m32 = 4 * tt + 2 * wm;
    char* vb = ws + OFF_V;
#pragma unroll
    for (int c = 0; c < 7; ++c) {
      int col = 16 * c + i16;
      int hh = col / 28, d = col - hh * 28;
      char* vp = vb + (size_t)(((h0 + hh) * 64 + m32) * 2 + (d >> 4)) * 1024 + (g * 16 + (d & 15)) * 16;
#pragma unroll
      for (int q = 0; q < 2; ++q) {
        u32x4 f = {pk2(acc[c][2 * q][0], acc[c][2 * q][1]), pk2(acc[c][2 * q][2], acc[c][2 * q][3]),
                   pk2(acc[c][2 * q + 1][0], acc[c][2 * q + 1][1]), pk2(acc[c][2 * q + 1][2], acc[c][2 * q + 1][3])};
        *(u32x4*)(vp + q * 2048) = f;
      }
    }
    if (i16 < 8) {
      int hh = i16 & 3, q = i16 >> 2;
      char* vp = vb + (size_t)(((h0 + hh) * 64 + m32 + q) * 2 + 1) * 1024 + (g * 16 + 12) * 16;
      *(u32x4*)vp = u32x4{0x3f803f80u, 0x3f803f80u, 0x3f803f80u, 0x3f803f80u};
    }
  }
}

DI float xmax16_32(float v) {
  auto a = __builtin_amdgcn_permlane16_swap(__float_as_uint(v), __float_as_uint(v), false, false);
  v = fmaxf(__uint_as_float(a[0]), __uint_as_float(a[1]));
  auto b = __builtin_amdgcn_permlane32_swap(__float_as_uint(v), __float_as_uint(v), false, false);
  return fmaxf(__uint_as_float(b[0]), __uint_as_float(b[1]));
}

__global__ __launch_bounds__(256, 1) void k2_attn(char* __restrict__ ws) {
  extern __shared__ __attribute__((aligned(16))) char smem[];
  const int tid = threadIdx.x, lane = tid & 63;
  const int w = __builtin_amdgcn_readfirstlane(tid >> 6);
  const int xcd = blockIdx.x & 7, idx = blockIdx.x >> 3;
  const int qb = 2 * xcd + (idx >> 4), h = idx & 15;
  const unsigned lds0 = (unsigned)(uintptr_t)(lds_vptr)smem;
  __amdgpu_buffer_rsrc_t rs = mkrsrc(ws, WS_BYTES);
#pragma unroll
  for (int u = 0; u < 6; ++u) {
    int i = 6 * w + u;
    int U = 8 * qb - 16 + i; U = U < 0 ? 0 : U;
    glds16(rs, lds0 + i * 1024, lane * 16, OFF_K + (h * 128 + U) * 1024);
  }
#pragma unroll
  for (int u = 0; u < 6; ++u) {
    int i = 6 * w + u;
    int m = 4 * qb - 8 + (i >> 1); m = m < 0 ? 0 : m;
    glds16(rs, lds0 + 24576 + i * 1024, lane * 16, OFF_V + ((h * 64 + m) * 2 + (i & 1)) * 1024);
  }
  const int T0 = 8 * qb + 2 * w;
  const int i16 = lane & 15, g = lane >> 4;
  bf16x8 q0 = *(const bf16x8*)(ws + OFF_Q + (size_t)(h * 128 + T0) * 1024 + lane * 16);
  bf16x8 q1 = *(const bf16x8*)(ws + OFF_Q + (size_t)(h * 128 + T0 + 1) * 1024 + lane * 16);
  const float* gp = (const float*)(ws + OFF_G);
  float g0 = gp[16 * T0 + i16], g1 = gp[16 * T0 + 16 + i16];
  WAITV(0);
  __syncthreads();
  const float NEG = -1e30f;
  f32x4 mlo, mhi;
#pragma unroll
  for (int r = 0; r < 4; ++r) { mlo[r] = (4 * g + r > i16) ? 0.f : NEG; mhi[r] = (4 * g + r <= i16) ? 0.f : NEG; }
  const f32x4 z4 = {0.f, 0.f, 0.f, 0.f};
  f32x4 s0[17], s1[17];
  const unsigned ka = lds0 + (2 * w) * 1024 + lane * 16;
#pragma unroll
  for (int i = 0; i < 18; ++i) {
    bf16x8 kf = lds_rd128(ka + i * 1024);
    if (i < 17) s0[i] = mfma16(kf, q0, i == 0 ? mlo : (i == 16 ? mhi : z4));
    if (i > 0) s1[i - 1] = mfma16(kf, q1, i == 1 ? mlo : (i == 17 ? mhi : z4));
  }
  if (T0 < 16) {
    const int first = 16 - T0;
#pragma unroll
    for (int i = 0; i < 16; ++i) {
      if (i < first) s0[i] = f32x4{NEG, NEG, NEG, NEG};
      if (i + 1 < first) s1[i] = f32x4{NEG, NEG, NEG, NEG};
    }
  }
  float m0 = NEG, m1 = NEG;
#pragma unroll
  for (int i = 0; i < 17; ++i) {
    m0 = fmaxf(fmaxf(m0, s0[i][0]), s0[i][1]); m0 = fmaxf(fmaxf(m0, s0[i][2]), s0[i][3]);
    m1 = fmaxf(fmaxf(m1, s1[i][0]), s1[i][1]); m1 = fmaxf(fmaxf(m1, s1[i][2]), s1[i][3]);
  }
  m0 = xmax16_32(m0); m1 = xmax16_32(m1);
  const float cs = 0.18898223650461357f * 1.4426950408889634f;
  const float n0 = -m0 * cs, n1 = -m1 * cs;
  unsigned p0[18][2], p1[18][2];
#pragma unroll
  for (int i = 0; i < 17; ++i) {
    p0[i][0] = pk2(__builtin_amdgcn_exp2f(fmaf(s0[i][0], cs, n0)), __builtin_amdgcn_exp2f(fmaf(s0[i][1], cs, n0)));
    p0[i][1] = pk2(__builtin_amdgcn_exp2f(fmaf(s0[i][2], cs, n0)), __builtin_amdgcn_exp2f(fmaf(s0[i][3], cs, n0)));
    p1[i + 1][0] = pk2(__builtin_amdgcn_exp2f(fmaf(s1[i][0], cs, n1)), __builtin_amdgcn_exp2f(fmaf(s1[i][1], cs, n1)));
    p1[i + 1][1] = pk2(__builtin_amdgcn_exp2f(fmaf(s1[i][2], cs, n1)), __builtin_amdgcn_exp2f(fmaf(s1[i][3], cs, n1)));
  }
  p0[17][0] = p0[17][1] = 0u; p1[0][0] = p1[0][1] = 0u;
  f32x4 o0[2] = {z4, z4}, o1[2] = {z4, z4};
  const unsigned va = lds0 + 24576 + (2 * w) * 1024 + lane * 16;
#pragma unroll
  for (int m = 0; m < 9; ++m) {
    bf16x8 b0 = __builtin_bit_cast(bf16x8, u32x4{p0[2 * m][0], p0[2 * m][1], p0[2 * m + 1][0], p0[2 * m + 1][1]});
    bf16x8 b1 = __builtin_bit_cast(bf16x8, u32x4{p1[2 * m][0], p1[2 * m][1], p1[2 * m + 1][0], p1[2 * m + 1][1]});
#pragma unroll
    for (int dt = 0; dt < 2; ++dt) {
      bf16x8 vf = lds_rd128(va + (2 * m + dt) * 1024);
      o0[dt] = mfma16(vf, b0, o0[dt]);
      o1[dt] = mfma16(vf, b1, o1[dt]);
    }
  }
  float l0 = __shfl(o0[1][0], 48 + i16), l1 = __shfl(o1[1][0], 48 + i16);
  float f0 = g0 / l0, f1 = g1 / l1;
  u32x4 r0 = {pk2(o0[0][0] * f0, o0[0][1] * f0), pk2(o0[0][2] * f0, o0[0][3] * f0), pk2(o0[1][0] * f0, o0[1][1] * f0), pk2(o0[1][2] * f0, o0[1][3] * f0)};
  u32x4 r1 = {pk2(o1[0][0] * f1, o1[0][1] * f1), pk2(o1[0][2] * f1, o1[0][3] * f1), pk2(o1[1][0] * f1, o1[1][1] * f1), pk2(o1[1][2] * f1, o1[1][3] * f1)};
  if (g == 3) { r0[2] = r0[3] = 0u; r1[2] = r1[3] = 0u; }
  *(u32x4*)(ws + OFF_O + (size_t)(T0 * 16 + h) * 1024 + lane * 16) = r0;
  *(u32x4*)(ws + OFF_O + (size_t)((T0 + 1) * 16 + h) * 1024 + lane * 16) = r1;
}

constexpr int K3_NB = 4, K3_LDS = 4 * K3_NB * 8192;

__global__ __launch_bounds__(256, 1) void k3_out(const unsigned short* __restrict__ w2, const unsigned short* __restrict__ wo,
                                                 char* __restrict__ ws, unsigned short* __restrict__ out) {
  extern __shared__ __attribute__((aligned(16))) char smem[];
  const int tid = threadIdx.x, lane = tid & 63;
  const int w = __builtin_amdgcn_readfirstlane(tid >> 6);
  const int xcd = blockIdx.x & 7, idx = blockIdx.x >> 3;
  const int t64 = 4 * xcd + idx / 7, nt = idx % 7;
  const unsigned lds0 = (unsigned)(uintptr_t)(lds_vptr)smem;
  const unsigned ldw = lds0 + w * (K3_NB * 8192);
  __amdgpu_buffer_rsrc_t rs = mkrsrc(ws, WS_BYTES);
  __amdgpu_buffer_rsrc_t r2 = mkrsrc(w2, FF * D_ * 2);
  __amdgpu_buffer_rsrc_t ro = mkrsrc(wo, D_ * D_ * 2);
  const int i16 = lane & 15, g = lane >> 4;
  const int r8 = lane >> 3;
  const unsigned vchunk = (unsigned)(((lane & 7) ^ (2 * ((r8 >> 1) & 3))) << 4) + nt * 128;
  const unsigned va = r8 * 896 + vchunk;
  const unsigned va3o = (r8 < 4 ? r8 : 3) * 896 + vchunk;
  const unsigned vl = lane * 16;
  auto issue = [&](int i) {
    unsigned lb = ldw + (i & (K3_NB - 1)) * 8192;
    if (i < 14) {
      int m = 14 * w + i;
#pragma unroll
      for (int u = 0; u < 4; ++u) glds16(r2, lb + u * 1024, va, (32 * m + 8 * u) * 896);
#pragma unroll
      for (int tk = 0; tk < 4; ++tk) glds16(rs, lb + 4096 + tk * 1024, vl, OFF_H + ((4 * t64 + tk) * 56 + m) * 1024);
    } else {
      int h = 4 * w + (i - 14);
#pragma unroll
      for (int u = 0; u < 4; ++u) glds16(ro, lb + u * 1024, u == 3 ? va3o : va, (28 * h + 8 * u) * 896);
#pragma unroll
      for (int tk = 0; tk < 4; ++tk) glds16(rs, lb + 4096 + tk * 1024, vl, OFF_O + ((4 * t64 + tk) * 16 + h) * 1024);
    }
  };
#pragma unroll
  for (int i = 0; i < K3_NB; ++i) issue(i);
  const float* gp = (const float*)(ws + OFF_G);
  float omg[4];
#pragma unroll
  for (int tk = 0; tk < 4; ++tk) omg[tk] = rbf(1.f - gp[64 * t64 + 16 * tk + i16]);
  f32x4 acc[4][4];
#pragma unroll
  for (int c = 0; c < 4; ++c)
#pragma unroll
    for (int tk = 0; tk < 4; ++tk) acc[c][tk] = f32x4{0.f, 0.f, 0.f, 0.f};
  const int trow = 4 * g + (i16 >> 2);
  unsigned ta[4];
#pragma unroll
  for (int c = 0; c < 4; ++c) ta[c] = trow * 128 + ((c ^ ((trow >> 1) & 3)) * 32) + 8 * (i16 & 3);
  for (int i = 0; i < 18; ++i) {
    if (i <= 18 - K3_NB) { WAITV(24); }
    else if (i == 15) { WAITV(16); }
    else if (i == 16) { WAITV(8); }
    else { WAITV(0); }
    unsigned lb = ldw + (i & (K3_NB - 1)) * 8192;
    bf16x8 af[4], bfr[4];
#pragma unroll
    for (int c = 0; c < 4; ++c) af[c] = lds_rdtr(lb + ta[c], lb + ta[c] + 2048);
#pragma unroll
    for (int tk = 0; tk < 4; ++tk) bfr[tk] = lds_rd128(lb + 4096 + tk * 1024 + lane * 16);
    WAITL0();
    SCHED0();
    if (i + K3_NB < 18) issue(i + K3_NB);
    if (i == 14) {
#pragma unroll
      for (int c = 0; c < 4; ++c)
#pragma unroll
        for (int tk = 0; tk < 4; ++tk) acc[c][tk] *= omg[tk];
    }
#pragma unroll
    for (int c = 0; c < 4; ++c)
#pragma unroll
      for (int tk = 0; tk < 4; ++tk) acc[c][tk] = mfma16(af[c], bfr[tk], acc[c][tk]);
  }
  __syncthreads();
#pragma unroll
  for (int c = 0; c < 4; ++c)
#pragma unroll
    for (int tk = 0; tk < 4; ++tk) *(lds_f32x4p)(uintptr_t)(lds0 + w * 16384 + (tk * 4 + c) * 1024 + lane * 16) = acc[c][tk];
  __syncthreads();
  const unsigned ob = lds0 + 65536 + w * 2048;
#pragma unroll
  for (int c = 0; c < 4; ++c) {
    f32x4 s = *(lds_f32x4p)(uintptr_t)(lds0 + (w * 4 + c) * 1024 + lane * 16);
#pragma unroll
    for (int ww = 1; ww < 4; ++ww) s += *(lds_f32x4p)(uintptr_t)(lds0 + ww * 16384 + (w * 4 + c) * 1024 + lane * 16);
    unsigned a = ob + i16 * 128 + (((2 * c + (g >> 1)) ^ (i16 & 7)) << 4) + (g & 1) * 8;
    *(lds_u32x2p)(uintptr_t)a = u32x2{pk2(s[0], s[1]), pk2(s[2], s[3])};
  }
  WAITL0();
#pragma unroll
  for (int u = 0; u < 2; ++u) {
    int row = 8 * u + (lane >> 3), ch = lane & 7;
    u32x4 v = *(lds_u32x4p)(uintptr_t)(ob + row * 128 + ((ch ^ (row & 7)) << 4));
    *(u32x4*)((char*)out + (size_t)(64 * t64 + 16 * w + row) * 896 + nt * 128 + ch * 16) = v;
  }
}

extern "C" void kernel_launch(void* const* d_in, const int* in_sizes, int n_in, void* d_out, int out_size, void* d_ws,
                              size_t ws_size, hipStream_t stream) {
  const unsigned short* x = (const unsigned short*)d_in[0];
  const unsigned short* wqkv = (const unsigned short*)d_in[1];
  const unsigned short* wo = (const unsigned short*)d_in[2];
  const unsigned short* w1 = (const unsigned short*)d_in[4];
  const unsigned short* w2 = (const unsigned short*)d_in[5];
  const unsigned short* gw = (const unsigned short*)d_in[6];
  char* ws = (char*)d_ws;
  (void)hipFuncSetAttribute((const void*)k1_gemm, hipFuncAttributeMaxDynamicSharedMemorySize, K1_LDS);
  (void)hipFuncSetAttribute((const void*)k3_out, hipFuncAttributeMaxDynamicSharedMemorySize, K3_LDS);
  k1_gemm<<<256, 256, K1_LDS, stream>>>(x, wqkv, w1, gw, ws);
  k2_attn<<<256, 256, 49152, stream>>>(ws);
  k3_out<<<224, 256, K3_LDS, stream>>>(w2, wo, ws, (unsigned short*)d_out);
}
```

```cpp
#include <hip/hip_runtime.h>
#include <stdint.h>
#include <utility>

typedef __attribute__((ext_vector_type(8))) __bf16 bf16x8;
typedef __attribute__((ext_vector_type(2))) __bf16 bf16x2;
typedef __attribute__((ext_vector_type(2))) float f32x2;
typedef __attribute__((ext_vector_type(4))) float f32x4;
typedef __attribute__((ext_vector_type(2))) short s16x2;
typedef __attribute__((ext_vector_type(4))) unsigned u32x4;
typedef __attribute__((ext_vector_type(2))) unsigned u32x2;
typedef __attribute__((address_space(3))) void* lds_vptr;
typedef __attribute__((address_space(3))) u32x4* lds_u32x4p;
typedef __attribute__((address_space(3))) u32x2* lds_u32x2p;
typedef __attribute__((address_space(3))) f32x4* lds_f32x4p;
typedef __attribute__((address_space(1))) unsigned gu32;
#define DI __device__ __forceinline__

#define K3ROT 0
#define K1MAP 1
#define K1PF 1
#define K1ROT 1
constexpr int S_ = 2048, D_ = 448, NH = 16, DH = 28, FF = 1792;
constexpr unsigned OFF_Q = 0u, OFF_K = 2u << 20, OFF_V = 4u << 20, OFF_O = 6u << 20, OFF_H = 8u << 20, OFF_G = 16u << 20, OFF_F = (16u << 20) + 65536u;
constexpr unsigned WS_BYTES = 17u << 20;

DI unsigned pk2(float a, float b) {
  f32x2 f = {a, b};
  return __builtin_bit_cast(unsigned, __builtin_convertvector(f, bf16x2));
}
DI unsigned relu2(unsigned p) { return __builtin_bit_cast(unsigned, __builtin_elementwise_max(__builtin_bit_cast(s16x2, p), s16x2{0, 0})); }
DI float rbf(float x) { return __uint_as_float(pk2(x, 0.f) << 16); }
DI f32x4 mfma16(bf16x8 a, bf16x8 b, f32x4 c) { return __builtin_amdgcn_mfma_f32_16x16x32_bf16(a, b, c, 0, 0, 0); }
DI __amdgpu_buffer_rsrc_t mkrsrc(const void* p, unsigned bytes) { return __builtin_amdgcn_make_buffer_rsrc((void*)p, 0, bytes, 0x00020000); }
DI void glds16(__amdgpu_buffer_rsrc_t rs, unsigned lds_addr, unsigned voff, unsigned soff) {
  __builtin_amdgcn_raw_ptr_buffer_load_lds(rs, (lds_vptr)(uintptr_t)lds_addr, 16, voff, soff, 0, 0);
}
#define DSR128(dst, addr, off) asm volatile("ds_read_b128 %0, %1 offset:%2" : "=v"(dst) : "v"(addr), "i"(off))
#define DSRTR(dst, addr, off) asm volatile("ds_read_b64_tr_b16 %0, %1 offset:%2" : "=v"(dst) : "v"(addr), "i"(off))
#define BLD128(dst, voff, rs, soff) asm volatile("buffer_load_dwordx4 %0, %1, %2, %3 offen" : "=v"(dst) : "v"(voff), "s"(rs), "s"(soff))
#define BLD128_SC1(dst, voff, rs, soff) asm volatile("buffer_load_dwordx4 %0, %1, %2, %3 offen sc1" : "=v"(dst) : "v"(voff), "s"(rs), "s"(soff))
#define BLD32(dst, voff, rs, soff) asm volatile("buffer_load_dword %0, %1, %2, %3 offen" : "=v"(dst) : "v"(voff), "s"(rs), "s"(soff))
#define BLD32_SC1(dst, voff, rs, soff) asm volatile("buffer_load_dword %0, %1, %2, %3 offen sc1" : "=v"(dst) : "v"(voff), "s"(rs), "s"(soff))
DI bf16x8 cat2(u32x2 lo, u32x2 hi) { return __builtin_bit_cast(bf16x8, __builtin_shufflevector(lo, hi, 0, 1, 2, 3)); }
DI bf16x8 asb(u32x4 v) { return __builtin_bit_cast(bf16x8, v); }
#define WAITV(n) asm volatile("s_waitcnt vmcnt(" #n ")" ::: "memory")
#define WAITL0() asm volatile("s_waitcnt lgkmcnt(0)" ::: "memory")
#define BAR() __builtin_amdgcn_s_barrier()
#define SCHED0() __builtin_amdgcn_sched_barrier(0)

#define ST_DECL
#define STAMP(i) do {} while (0)
#define ST_FLUSH(k) do {} while (0)
#define ST_ARG
#define ST_PASS

constexpr int K1_XB = 16384, K1_WB = 28672, K1_STAGE = K1_XB + K1_WB, K1_LDS = 3 * K1_STAGE;

struct K1Frag { u32x4 x[4]; u32x2 wl[7], wh[7]; };
#define K1_TIE(F) "+v"(F.x[0]), "+v"(F.x[1]), "+v"(F.x[2]), "+v"(F.x[3]), "+v"(F.wl[0]), "+v"(F.wl[1]), "+v"(F.wl[2]), "+v"(F.wl[3]), \
  "+v"(F.wl[4]), "+v"(F.wl[5]), "+v"(F.wl[6]), "+v"(F.wh[0]), "+v"(F.wh[1]), "+v"(F.wh[2]), "+v"(F.wh[3]), "+v"(F.wh[4]), "+v"(F.wh[5]), "+v"(F.wh[6])

template <int I, int KS>
DI void k1_rd1(K1Frag& F, unsigned xa, unsigned we, unsigned wo_) {
  if constexpr (I < 4) { DSR128(F.x[I], xa, I * 2048); }
  else if constexpr (I < 18) {
    constexpr int c = (I - 4) >> 1, hi = (I - 4) & 1, o = KS * 32 * 448 + c * 32 + hi * 1792;
    if constexpr (hi) { if constexpr (c & 1) DSRTR(F.wh[c], wo_, o); else DSRTR(F.wh[c], we, o); }
    else { if constexpr (c & 1) DSRTR(F.wl[c], wo_, o); else DSRTR(F.wl[c], we, o); }
  }
}
template <bool ISV, int I>
DI void k1_mm1(f32x4 (&acc)[7][4], const K1Frag& F) {
  constexpr int c = I >> 2, mt = I & 3;
  bf16x8 wf = cat2(F.wl[c], F.wh[c]);
  acc[c][mt] = ISV ? mfma16(asb(F.x[mt]), wf, acc[c][mt]) : mfma16(wf, asb(F.x[mt]), acc[c][mt]);
}
template <bool ISV, int KS, int... Is>
DI void k1_step(f32x4 (&acc)[7][4], const K1Frag& C, K1Frag& N, unsigned xa, unsigned we, unsigned wo_, std::integer_sequence<int, Is...>) {
  ((k1_mm1<ISV, Is>(acc, C), k1_rd1<Is, KS>(N, xa, we, wo_), SCHED0()), ...);
}
template <int KS, int... Is>
DI void k1_rd_all(K1Frag& N, unsigned xa, unsigned we, unsigned wo_, std::integer_sequence<int, Is...>) {
  (k1_rd1<Is, KS>(N, xa, we, wo_), ...);
}

template <bool ISV>
DI void k1_compute(f32x4 (&acc)[7][4], unsigned xa0, unsigned xa1, unsigned wa_e, unsigned wa_o ST_ARG) {
  K1Frag F0, F1;
  BAR();
  STAMP(3);
  k1_rd_all<0>(F0, xa0, wa_e, wa_o, std::make_integer_sequence<int, 18>());
  int buf = 0;
#pragma unroll 1
  for (int s = 0; s < 7; ++s) {
    unsigned bo = buf * K1_STAGE;
    asm volatile("s_waitcnt lgkmcnt(0)" : K1_TIE(F0));
    k1_step<ISV, 1>(acc, F0, F1, xa1 + bo, wa_e + bo, wa_o + bo, std::make_integer_sequence<int, 28>());
    int nb = buf == 2 ? 0 : buf + 1;
    asm volatile("s_waitcnt lgkmcnt(0)" : K1_TIE(F1) :: "memory");
    BAR();
    unsigned bn = nb * K1_STAGE;
    k1_step<ISV, 0>(acc, F1, F0, xa0 + bn, wa_e + bn, wa_o + bn, std::make_integer_sequence<int, 28>());
    buf = nb;
  }
  asm volatile("s_waitcnt lgkmcnt(0)" : K1_TIE(F0));
  STAMP(10);
}

__global__ __launch_bounds__(512, 2) void k1_gemm(const unsigned short* __restrict__ x, const unsigned short* __restrict__ wqkv,
                                                  const unsigned short* __restrict__ w1, const unsigned short* __restrict__ gw,
                                                  const unsigned short* __restrict__ w2, const unsigned short* __restrict__ wo,
                                                  char* __restrict__ ws) {
  extern __shared__ __attribute__((aligned(16))) char smem[];
  const int tid = threadIdx.x, lane = tid & 63;
  const int w = __builtin_amdgcn_readfirstlane(tid >> 6);
  const int xcd = blockIdx.x & 7, idx = blockIdx.x >> 3;
  ST_DECL
  STAMP(0);
  if (idx >= 28) {
    const int gb = xcd * 4 + (idx - 28);
    if (tid < 8) *(unsigned*)(ws + OFF_F + (gb * 8 + tid) * 128) = 0u;
    const int t0 = gb * 64 + w * 8;
    u32x4 gv = {0, 0, 0, 0};
    if (lane < 56) gv = *(const u32x4*)(gw + lane * 8);
    float* g = (float*)(ws + OFF_G);
    u32x4 xv[8];
#pragma unroll
    for (int t = 0; t < 8; ++t) {
      xv[t] = u32x4{0, 0, 0, 0};
      if (lane < 56) xv[t] = *(const u32x4*)(x + (size_t)(t0 + t) * D_ + lane * 8);
    }
    float mine = 0.f;
#pragma unroll
    for (int t = 0; t < 8; ++t) {
      float s = 0.f;
#pragma unroll
      for (int j = 0; j < 4; ++j) {
        s += rbf(__uint_as_float(xv[t][j] << 16) * __uint_as_float(gv[j] << 16));
        s += rbf(__uint_as_float(xv[t][j] & 0xffff0000u) * __uint_as_float(gv[j] & 0xffff0000u));
      }
#pragma unroll
      for (int o = 32; o > 0; o >>= 1) s += __shfl_xor(s, o);
      if (lane == t) mine = s;
    }
    float z = rbf(mine);
    float gg = rbf(1.f / (1.f + __expf(-z)));
    if (lane < 8) g[t0 + lane] = gg;
    STAMP(15);
    ST_FLUSH(0);
    return;
  }
  const int tt = 2 * xcd + (idx >= 14 ? 1 : 0), ct = idx >= 14 ? idx - 14 : idx;
  const bool isq = ct < 6;
  const unsigned lds0 = (unsigned)(uintptr_t)(lds_vptr)smem;
  if (w >= 4) {
    const int lw = w - 4;
    const unsigned strideB = isq ? 2688u : 3584u;
    const unsigned col0B = (isq ? ct : ct - 6) * 448u;
    __amdgpu_buffer_rsrc_t rx = mkrsrc(x, S_ * D_ * 2);
    __amdgpu_buffer_rsrc_t rw = mkrsrc(isq ? wqkv : w1, isq ? D_ * 3 * D_ * 2 : D_ * FF * 2);
    unsigned vx[2], vw[2];
#pragma unroll
    for (int u = 0; u < 2; ++u) vx[u] = (lane >> 3) * 896 + (((lane & 7) ^ (((lane >> 4) + 4 * u) & 7)) << 4);
    { const int hi = lane >= 28 ? 1 : 0, p = lane - 28 * hi;
      vw[0] = hi * strideB + p * 16; vw[1] = hi * strideB + (p ^ 2) * 16; }
    const unsigned sx0 = (unsigned)(128 * tt + 32 * lw) * 896u;
    const unsigned sw0 = col0B + 16 * lw * strideB;
    const unsigned wstep = 64 * strideB;
    auto issue = [&](int s, int buf) {
      const unsigned lb = lds0 + buf * K1_STAGE;
#pragma unroll
      for (int u = 0; u < 4; ++u) glds16(rx, lb + (4 * lw + u) * 1024, vx[u & 1], sx0 + s * 128 + u * (8 * 896));
      if (lane < 56) {
#pragma unroll
        for (int u = 0; u < 8; ++u) glds16(rw, lb + K1_XB + (8 * lw + u) * 896, vw[(u >> 2) & 1], sw0 + s * wstep + 2 * u * strideB);
      }
    };
    issue(0, 0); issue(1, 1);
    STAMP(1);
    WAITV(12); BAR();
    STAMP(2);
    issue(2, 2);
    int buf = 0;
#pragma unroll 1
    for (int s = 0; s < 4; ++s) {
      WAITV(12);
      BAR();
      issue(s + 3, buf);
      buf = buf == 2 ? 0 : buf + 1;
    }
    WAITV(12);
    BAR();
    { const unsigned pb = lds0 + 1 * K1_STAGE + lw * 11264;
      const __amdgpu_buffer_rsrc_t r2 = mkrsrc(w2, FF * D_ * 2), ro = mkrsrc(wo, D_ * D_ * 2);
      const int slice = idx * 4 + lw;
#pragma unroll
      for (int u = 0; u < 14; ++u) glds16(r2, pb + (u % 11) * 1024, lane * 16, (slice * 14 + u) * 1024);
#pragma unroll
      for (int u = 0; u < 4; ++u) glds16(ro, pb + u * 1024, lane * 16, slice * 3584 + u * 1024);
    }
    WAITV(18);
    BAR();
    BAR();
    ST_FLUSH(0);
    return;
  }
  const int wm = w & 1, wn = w >> 1;
  const int i16 = lane & 15, g = lane >> 4;
  const unsigned xrow = (64 * wm + i16) * 128;
  const unsigned xa0 = lds0 + xrow + (((g) ^ (i16 >> 1)) << 4);
  const unsigned xa1 = lds0 + xrow + (((4 + g) ^ (i16 >> 1)) << 4);
  const unsigned wbase = lds0 + K1_XB + (8 * g + (i16 >> 2)) * 448 + 8 * (i16 & 3) + 7 * wn * 32;
  const int b = g & 1;
  const unsigned wa_e = wbase + ((wn & 1) ? -32 * b : 32 * b);
  const unsigned wa_o = wbase + ((wn & 1) ? 32 * b : -32 * b);
  f32x4 acc[7][4];
#pragma unroll
  for (int c = 0; c < 7; ++c)
#pragma unroll
    for (int m = 0; m < 4; ++m) acc[c][m] = f32x4{0.f, 0.f, 0.f, 0.f};
  const bool isv = (ct == 4 || ct == 5);
  if (isv) k1_compute<true>(acc, xa0, xa1, wa_e, wa_o ST_PASS);
  else k1_compute<false>(acc, xa0, xa1, wa_e, wa_o ST_PASS);

  const int t16 = 8 * tt + 4 * wm;
  if (ct >= 6) {
    char* hb = ws + OFF_H + lane * 16;
    const int m0 = 7 * (ct - 6);
#pragma unroll
    for (int mt = 0; mt < 4; ++mt) {
      unsigned p[7][2];
#pragma unroll
      for (int c = 0; c < 7; ++c) {
        p[c][0] = relu2(pk2(acc[c][mt][0], acc[c][mt][1]));
        p[c][1] = relu2(pk2(acc[c][mt][2], acc[c][mt][3]));
      }
      char* hp = hb + (size_t)((t16 + mt) * 56 + m0) * 1024;
      if (wn == 0) {
#pragma unroll
        for (int q = 0; q < 3; ++q) *(u32x4*)(hp + q * 1024) = u32x4{p[2 * q][0], p[2 * q][1], p[2 * q + 1][0], p[2 * q + 1][1]};
        *(u32x2*)(hp + 3 * 1024) = u32x2{p[6][0], p[6][1]};
      } else {
        *(u32x2*)(hp + 3 * 1024 + 8) = u32x2{p[0][0], p[0][1]};
#pragma unroll
        for (int q = 0; q < 3; ++q) *(u32x4*)(hp + (4 + q) * 1024) = u32x4{p[2 * q + 1][0], p[2 * q + 1][1], p[2 * q + 2][0], p[2 * q + 2][1]};
      }
    }
  } else if (!isv) {
    const int h0 = (ct & 1) * 8 + wn * 4;
    char* qb = ws + (ct < 2 ? OFF_Q : OFF_K) + lane * 16;
#pragma unroll
    for (int mt = 0; mt < 4; ++mt) {
      unsigned p[7][2];
#pragma unroll
      for (int c = 0; c < 7; ++c) {
        p[c][0] = pk2(acc[c][mt][0], acc[c][mt][1]);
        p[c][1] = pk2(acc[c][mt][2], acc[c][mt][3]);
      }
      u32x4 f0 = {p[0][0], p[0][1], g < 3 ? p[1][0] : 0u, g < 3 ? p[1][1] : 0u};
      u32x4 f1 = {p[2][0], p[2][1], g < 2 ? p[3][0] : (g == 2 ? 0u : p[1][0]), g < 2 ? p[3][1] : (g == 2 ? 0u : p[1][1])};
      u32x4 f2 = {p[4][0], p[4][1], g == 0 ? p[5][0] : (g == 1 ? 0u : p[3][0]), g == 0 ? p[5][1] : (g == 1 ? 0u : p[3][1])};
      u32x4 f3 = {p[6][0], p[6][1], g == 0 ? 0u : p[5][0], g == 0 ? 0u : p[5][1]};
      char* qp = qb + (size_t)(h0 * 128 + t16 + mt) * 1024;
      *(u32x4*)(qp) = f0;
      *(u32x4*)(qp + 128 * 1024) = f1;
      *(u32x4*)(qp + 2 * 128 * 1024) = f2;
      *(u32x4*)(qp + 3 * 128 * 1024) = f3;
    }
  } else {
    const int h0 = (ct & 1) * 8 + wn * 4;
    const int m32 = 4 * tt + 2 * wm;
    char* vb = ws + OFF_V;
#pragma unroll
    for (int c = 0; c < 7; ++c) {
      int col = 16 * c + i16;
      int hh = col / 28, d = col - hh * 28;
      char* vp = vb + (size_t)(((h0 + hh) * 64 + m32) * 2 + (d >> 4)) * 1024 + (g * 16 + (d & 15)) * 16;
#pragma unroll
      for (int q = 0; q < 2; ++q) {
        u32x4 f = {pk2(acc[c][2 * q][0], acc[c][2 * q][1]), pk2(acc[c][2 * q][2], acc[c][2 * q][3]),
                   pk2(acc[c][2 * q + 1][0], acc[c][2 * q + 1][1]), pk2(acc[c][2 * q + 1][2], acc[c][2 * q + 1][3])};
        *(u32x4*)(vp + q * 2048) = f;
      }
    }
    if (i16 < 8) {
      int hh = i16 & 3, q = i16 >> 2;
      char* vp = vb + (size_t)(((h0 + hh) * 64 + m32 + q) * 2 + 1) * 1024 + (g * 16 + 12) * 16;
      *(u32x4*)vp = u32x4{0x3f803f80u, 0x3f803f80u, 0x3f803f80u, 0x3f803f80u};
    }
  }
  STAMP(11);
  STAMP(12);
  ST_FLUSH(0);
}

constexpr int K23_RING = 49152, K23_LDS = K23_RING + 8 * 3 * 4096;

DI float xmax16_32(float v) {
  auto a = __builtin_amdgcn_permlane16_swap(__float_as_uint(v), __float_as_uint(v), false, false);
  v = fmaxf(__uint_as_float(a[0]), __uint_as_float(a[1]));
  auto b = __builtin_amdgcn_permlane32_swap(__float_as_uint(v), __float_as_uint(v), false, false);
  return fmaxf(__uint_as_float(b[0]), __uint_as_float(b[1]));
}
struct BFrag { u32x4 b[4]; };
#define B_TIE(B) "+v"(B.b[0]), "+v"(B.b[1]), "+v"(B.b[2]), "+v"(B.b[3])

struct K3Ctx {
  __amdgpu_buffer_rsrc_t rs, r2, ro;
  unsigned ldw;
  unsigned va, va3o, vl;
  unsigned sA, sB;
  unsigned sAo, sBo;
  unsigned ta[4];
};
template <int I>
DI void k3_issueA(const K3Ctx& c) {
  constexpr int slot = I % 3;
  if constexpr (I < 7) {
#pragma unroll
    for (int u = 0; u < 4; ++u) glds16(c.r2, c.ldw + slot * 4096 + u * 1024, c.va, c.sA + (32 * I + 8 * u) * 896);
  } else {
#pragma unroll
    for (int u = 0; u < 4; ++u) glds16(c.ro, c.ldw + slot * 4096 + u * 1024, u == 3 ? c.va3o : c.va, c.sAo + (28 * (I - 7) + 8 * u) * 896);
  }
}
template <int I>
DI void k3_issueB(const K3Ctx& c, BFrag& B) {
  if constexpr (I < 7) {
    BLD128(B.b[0], c.vl, c.rs, c.sB + I * 1024);
    BLD128(B.b[1], c.vl, c.rs, c.sB + I * 1024 + 56 * 1024);
    BLD128(B.b[2], c.vl, c.rs, c.sB + I * 1024 + 2 * 56 * 1024);
    BLD128(B.b[3], c.vl, c.rs, c.sB + I * 1024 + 3 * 56 * 1024);
  } else {
    BLD128_SC1(B.b[0], c.vl, c.rs, c.sBo + (I - 7) * 1024);
    BLD128_SC1(B.b[1], c.vl, c.rs, c.sBo + (I - 7) * 1024 + 16 * 1024);
    BLD128_SC1(B.b[2], c.vl, c.rs, c.sBo + (I - 7) * 1024 + 2 * 16 * 1024);
    BLD128_SC1(B.b[3], c.vl, c.rs, c.sBo + (I - 7) * 1024 + 3 * 16 * 1024);
  }
}
template <int I, bool REFILL>
DI void k3_step(const K3Ctx& c, f32x4 (&acc)[4][4], BFrag& B) {
  constexpr int so = (I % 3) * 4096;
  u32x2 al[4], ah[4];
  DSRTR(al[0], c.ta[0], so); DSRTR(ah[0], c.ta[0], so + 2048); DSRTR(al[1], c.ta[1], so); DSRTR(ah[1], c.ta[1], so + 2048);
  DSRTR(al[2], c.ta[2], so); DSRTR(ah[2], c.ta[2], so + 2048); DSRTR(al[3], c.ta[3], so); DSRTR(ah[3], c.ta[3], so + 2048);
  asm volatile("s_waitcnt lgkmcnt(0)" : "+v"(al[0]), "+v"(al[1]), "+v"(al[2]), "+v"(al[3]), "+v"(ah[0]), "+v"(ah[1]), "+v"(ah[2]), "+v"(ah[3]) :: "memory");
  if constexpr (REFILL) k3_issueA<I + 3>(c);
  SCHED0();
#pragma unroll
  for (int cc = 0; cc < 4; ++cc)
#pragma unroll
    for (int tk = 0; tk < 4; ++tk) acc[cc][tk] = mfma16(cat2(al[cc], ah[cc]), asb(B.b[tk]), acc[cc][tk]);
  SCHED0();
}

__global__ __launch_bounds__(512, 2) void k23(const unsigned short* __restrict__ w2, const unsigned short* __restrict__ wo,
                                              char* __restrict__ ws, unsigned short* __restrict__ out) {
  extern __shared__ __attribute__((aligned(16))) char smem[];
  const int tid = threadIdx.x, lane = tid & 63;
  const int w = __builtin_amdgcn_readfirstlane(tid >> 6);
  const int xcd = blockIdx.x & 7, idx = blockIdx.x >> 3;
  const int qb = 2 * xcd + (idx >> 4), h = idx & 15;
  const bool has3 = idx < 28;
  const int t64 = 4 * xcd + (has3 ? idx / 7 : 0), nt = has3 ? idx % 7 : 0;
  ST_DECL
  STAMP(0);
  const unsigned lds0 = (unsigned)(uintptr_t)(lds_vptr)smem;
  const int i16 = lane & 15, g = lane >> 4;
  K3Ctx c;
  c.rs = mkrsrc(ws, WS_BYTES);
  c.vl = lane * 16;
#pragma unroll
  for (int u = 0; u < 3; ++u) {
    int i = 3 * w + u;
    int U = 8 * qb - 16 + i; U = U < 0 ? 0 : U;
    glds16(c.rs, lds0 + i * 1024, c.vl, OFF_K + (h * 128 + U) * 1024);
  }
#pragma unroll
  for (int u = 0; u < 3; ++u) {
    int i = 3 * w + u;
    int m = 4 * qb - 8 + (i >> 1); m = m < 0 ? 0 : m;
    glds16(c.rs, lds0 + 24576 + i * 1024, c.vl, OFF_V + ((h * 64 + m) * 2 + (i & 1)) * 1024);
  }
  const int T = 8 * qb + w;
  u32x4 qf; unsigned gq, g3[4];
  BLD128(qf, c.vl, c.rs, OFF_Q + (h * 128 + T) * 1024);
  { unsigned vg = i16 * 4;
    BLD32(gq, vg, c.rs, OFF_G + T * 64);
    BLD32(g3[0], vg, c.rs, OFF_G + t64 * 256); BLD32(g3[1], vg, c.rs, OFF_G + t64 * 256 + 64);
    BLD32(g3[2], vg, c.rs, OFF_G + t64 * 256 + 128); BLD32(g3[3], vg, c.rs, OFF_G + t64 * 256 + 192); }
  c.r2 = mkrsrc(w2, FF * D_ * 2);
  c.ro = mkrsrc(wo, D_ * D_ * 2);
  c.ldw = lds0 + K23_RING + w * (3 * 4096);
  { const int r8 = lane >> 3;
    const unsigned vchunk = (unsigned)(((lane & 7) ^ (2 * ((r8 >> 1) & 3))) << 4) + nt * 128;
    c.va = r8 * 896 + vchunk;
    c.va3o = (r8 < 4 ? r8 : 3) * 896 + vchunk; }
  c.sA = (unsigned)(7 * w) * 32 * 896;
  c.sB = OFF_H + (unsigned)((4 * t64) * 56 + 7 * w) * 1024;
  c.sAo = (unsigned)(2 * w) * 28 * 896;
  c.sBo = OFF_O + (unsigned)((4 * t64) * 16 + 2 * w) * 1024;
  BFrag B0, B1, B2;
  STAMP(1);
  asm volatile("s_waitcnt vmcnt(0)" : "+v"(qf), "+v"(gq), "+v"(g3[0]), "+v"(g3[1]), "+v"(g3[2]), "+v"(g3[3]) :: "memory");
  BAR();
  STAMP(2);
  if (has3) { k3_issueA<0>(c); k3_issueB<0>(c, B0); }
  {
    const float NEG = -1e30f;
    const bool odd = w & 1;
    f32x4 c0, c1, c16, c17;
#pragma unroll
    for (int r = 0; r < 4; ++r) {
      float lo = (4 * g + r > i16) ? 0.f : NEG, hi = (4 * g + r <= i16) ? 0.f : NEG;
      c0[r] = odd ? NEG : lo; c1[r] = odd ? lo : 0.f; c16[r] = odd ? 0.f : hi; c17[r] = odd ? hi : NEG;
    }
    const f32x4 z4 = {0.f, 0.f, 0.f, 0.f};
    f32x4 s[18];
    const char* kp = smem + (w & ~1) * 1024 + lane * 16;
    const bf16x8 q = asb(qf);
#pragma unroll
    for (int i = 0; i < 18; ++i) {
      bf16x8 kf = *(const bf16x8*)(kp + i * 1024);
      s[i] = mfma16(kf, q, i == 0 ? c0 : (i == 1 ? c1 : (i == 16 ? c16 : (i == 17 ? c17 : z4))));
    }
    if (qb < 2) {
      const int first = 16 - 8 * qb - (w & ~1);
#pragma unroll
      for (int i = 0; i < 16; ++i)
        if (i < first) s[i] = f32x4{NEG, NEG, NEG, NEG};
    }
    STAMP(3);
    if (has3) { k3_issueA<1>(c); k3_issueB<1>(c, B1); }
    float m0 = NEG;
#pragma unroll
    for (int i = 0; i < 18; ++i) { m0 = fmaxf(fmaxf(m0, s[i][0]), s[i][1]); m0 = fmaxf(fmaxf(m0, s[i][2]), s[i][3]); }
    m0 = xmax16_32(m0);
    const float cs = 0.18898223650461357f * 1.4426950408889634f;
    const float n0 = -m0 * cs;
    f32x4 o[2] = {z4, z4};
    const char* vp = smem + 24576 + (w & ~1) * 1024 + lane * 16;
#pragma unroll
    for (int m = 0; m < 9; ++m) {
      u32x4 p;
#pragma unroll
      for (int t = 0; t < 2; ++t) {
        const f32x4 v = s[2 * m + t];
        p[2 * t] = pk2(__builtin_amdgcn_exp2f(fmaf(v[0], cs, n0)), __builtin_amdgcn_exp2f(fmaf(v[1], cs, n0)));
        p[2 * t + 1] = pk2(__builtin_amdgcn_exp2f(fmaf(v[2], cs, n0)), __builtin_amdgcn_exp2f(fmaf(v[3], cs, n0)));
      }
#pragma unroll
      for (int dt = 0; dt < 2; ++dt) o[dt] = mfma16(*(const bf16x8*)(vp + (2 * m + dt) * 1024), asb(p), o[dt]);
    }
    STAMP(4);
    float l = __shfl(o[1][0], 48 + i16);
    float f = __uint_as_float(gq) / l;
    u32x4 r = {pk2(o[0][0] * f, o[0][1] * f), pk2(o[0][2] * f, o[0][3] * f), pk2(o[1][0] * f, o[1][1] * f), pk2(o[1][2] * f, o[1][3] * f)};
    if (g == 3) { r[2] = 0u; r[3] = 0u; }
    __builtin_amdgcn_raw_buffer_store_b128(r, c.rs, c.vl, OFF_O + (T * 16 + h) * 1024, 16);
  }
  WAITV(0);
  BAR();
  if (tid == 0) __hip_atomic_store((gu32*)(ws + OFF_F + (qb * 16 + h) * 128), 1u, __ATOMIC_RELAXED, __HIP_MEMORY_SCOPE_AGENT);
  STAMP(5);
  if (!has3) { ST_FLUSH(1); return; }
  f32x4 acc[4][4];
#pragma unroll
  for (int cc = 0; cc < 4; ++cc)
#pragma unroll
    for (int tk = 0; tk < 4; ++tk) acc[cc][tk] = f32x4{0.f, 0.f, 0.f, 0.f};
  { const int trow = 4 * g + (i16 >> 2);
#pragma unroll
    for (int cc = 0; cc < 4; ++cc) c.ta[cc] = c.ldw + trow * 128 + ((cc ^ ((trow >> 1) & 3)) * 32) + 8 * (i16 & 3); }
  k3_issueA<2>(c); k3_issueB<2>(c, B2);
  asm volatile("" : B_TIE(B0), B_TIE(B1));
  k3_step<0, true>(c, acc, B0); k3_issueB<3>(c, B0);
  k3_step<1, true>(c, acc, B1); k3_issueB<4>(c, B1);
  asm volatile("s_waitcnt vmcnt(16)" : B_TIE(B2) :: "memory");
  k3_step<2, true>(c, acc, B2); k3_issueB<5>(c, B2);
  unsigned fl;
  const unsigned vf = (lane & 1) * 128, sf = OFF_F + (unsigned)((t64 >> 1) * 16 + 2 * w) * 128;
  BLD32_SC1(fl, vf, c.rs, sf);
  STAMP(6);
  asm volatile("s_waitcnt vmcnt(17)" : B_TIE(B0) :: "memory");
  k3_step<3, true>(c, acc, B0); k3_issueB<6>(c, B0);
  asm volatile("s_waitcnt vmcnt(17)" : B_TIE(B1) :: "memory");
  k3_step<4, true>(c, acc, B1);
  asm volatile("s_waitcnt vmcnt(12)" : "+v"(fl), B_TIE(B2) :: "memory");
  for (unsigned spins = 0; !__all(fl != 0u) && spins < (1u << 22); ++spins) {
    __builtin_amdgcn_s_sleep(1);
    BLD32_SC1(fl, vf, c.rs, sf);
    asm volatile("s_waitcnt vmcnt(0)" : "+v"(fl) :: "memory");
  }
  k3_issueB<7>(c, B1);
  k3_step<5, true>(c, acc, B2); k3_issueB<8>(c, B2);
  asm volatile("s_waitcnt vmcnt(16)" : B_TIE(B0) :: "memory");
  k3_step<6, false>(c, acc, B0);
  STAMP(7);
#pragma unroll
  for (int tk = 0; tk < 4; ++tk) {
    float omg = rbf(1.f - __uint_as_float(g3[tk]));
#pragma unroll
    for (int cc = 0; cc < 4; ++cc) acc[cc][tk] *= omg;
  }
  asm volatile("s_waitcnt vmcnt(8)" : B_TIE(B1) :: "memory");
  k3_step<7, false>(c, acc, B1);
  asm volatile("s_waitcnt vmcnt(0)" : B_TIE(B2) :: "memory");
  k3_step<8, false>(c, acc, B2);
  STAMP(8);
  BAR();
#pragma unroll
  for (int cc = 0; cc < 4; ++cc)
#pragma unroll
    for (int tk = 0; tk < 4; ++tk) *(lds_f32x4p)(uintptr_t)(lds0 + w * 16384 + (tk * 4 + cc) * 1024 + lane * 16) = acc[cc][tk];
  __syncthreads();
  STAMP(9);
  const unsigned ob = lds0 + 131072;
#pragma unroll
  for (int j = 0; j < 2; ++j) {
    const int tile = 2 * w + j, tk = tile >> 2, cc = tile & 3;
    f32x4 s = *(lds_f32x4p)(uintptr_t)(lds0 + tile * 1024 + lane * 16);
#pragma unroll
    for (int ww = 1; ww < 8; ++ww) s += *(lds_f32x4p)(uintptr_t)(lds0 + ww * 16384 + tile * 1024 + lane * 16);
    const int row = 16 * tk + i16;
    unsigned a = ob + row * 128 + (((2 * cc + (g >> 1)) ^ (row & 7)) << 4) + (g & 1) * 8;
    *(lds_u32x2p)(uintptr_t)a = u32x2{pk2(s[0], s[1]), pk2(s[2], s[3])};
  }
  __syncthreads();
  { const int row = 8 * w + (lane >> 3), ch = lane & 7;
    u32x4 v = *(lds_u32x4p)(uintptr_t)(ob + row * 128 + ((ch ^ (row & 7)) << 4));
    *(u32x4*)((char*)out + (size_t)(64 * t64 + row) * 896 + nt * 128 + ch * 16) = v; }
  STAMP(10);
  STAMP(11);
  ST_FLUSH(1);
}

extern "C" void kernel_launch(void* const* d_in, const int* in_sizes, int n_in, void* d_out, int out_size, void* d_ws,
                              size_t ws_size, hipStream_t stream) {
  const unsigned short* x = (const unsigned short*)d_in[0];
  const unsigned short* wqkv = (const unsigned short*)d_in[1];
  const unsigned short* wo = (const unsigned short*)d_in[2];
  const unsigned short* w1 = (const unsigned short*)d_in[4];
  const unsigned short* w2 = (const unsigned short*)d_in[5];
  const unsigned short* gw = (const unsigned short*)d_in[6];
  char* ws = (char*)d_ws;
  (void)hipFuncSetAttribute((const void*)k1_gemm, hipFuncAttributeMaxDynamicSharedMemorySize, K1_LDS);
  (void)hipFuncSetAttribute((const void*)k23, hipFuncAttributeMaxDynamicSharedMemorySize, K23_LDS);
  k1_gemm<<<256, 512, K1_LDS, stream>>>(x, wqkv, w1, gw, w2, wo, ws);
  k23<<<256, 512, K23_LDS, stream>>>(w2, wo, ws, (unsigned short*)d_out);
}
```

```cpp
#include <hip/hip_runtime.h>
#include <stdint.h>
#include <utility>

typedef __attribute__((ext_vector_type(8))) __bf16 bf16x8;
typedef __attribute__((ext_vector_type(2))) __bf16 bf16x2;
typedef __attribute__((ext_vector_type(2))) float f32x2;
typedef __attribute__((ext_vector_type(4))) float f32x4;
typedef __attribute__((ext_vector_type(2))) short s16x2;
typedef __attribute__((ext_vector_type(4))) unsigned u32x4;
typedef __attribute__((ext_vector_type(2))) unsigned u32x2;
typedef __attribute__((address_space(3))) void* lds_vptr;
typedef __attribute__((address_space(3))) u32x4* lds_u32x4p;
typedef __attribute__((address_space(3))) u32x2* lds_u32x2p;
typedef __attribute__((address_space(3))) f32x4* lds_f32x4p;
typedef __attribute__((address_space(1))) unsigned gu32;
#define DI __device__ __forceinline__

#define K3ROT 0
#define K1MAP 1
#define K1PF 1
#define K1ROT 1
constexpr int S_ = 2048, D_ = 448, NH = 16, DH = 28, FF = 1792;
constexpr unsigned OFF_Q = 0u, OFF_K = 2u << 20, OFF_V = 4u << 20, OFF_O = 6u << 20, OFF_H = 8u << 20, OFF_G = 16u << 20, OFF_F = (16u << 20) + 65536u;
constexpr unsigned WS_BYTES = 17u << 20;

DI unsigned pk2(float a, float b) {
  f32x2 f = {a, b};
  return __builtin_bit_cast(unsigned, __builtin_convertvector(f, bf16x2));
}
DI unsigned relu2(unsigned p) { return __builtin_bit_cast(unsigned, __builtin_elementwise_max(__builtin_bit_cast(s16x2, p), s16x2{0, 0})); }
DI float rbf(float x) { return __uint_as_float(pk2(x, 0.f) << 16); }
DI f32x4 mfma16(bf16x8 a, bf16x8 b, f32x4 c) { return __builtin_amdgcn_mfma_f32_16x16x32_bf16(a, b, c, 0, 0, 0); }
DI __amdgpu_buffer_rsrc_t mkrsrc(const void* p, unsigned bytes) { return __builtin_amdgcn_make_buffer_rsrc((void*)p, 0, bytes, 0x00020000); }
DI void glds16(__amdgpu_buffer_rsrc_t rs, unsigned lds_addr, unsigned voff, unsigned soff) {
  __builtin_amdgcn_raw_ptr_buffer_load_lds(rs, (lds_vptr)(uintptr_t)lds_addr, 16, voff, soff, 0, 0);
}
#define DSR128(dst, addr, off) asm volatile("ds_read_b128 %0, %1 offset:%2" : "=v"(dst) : "v"(addr), "i"(off))
#define DSRTR(dst, addr, off) asm volatile("ds_read_b64_tr_b16 %0, %1 offset:%2" : "=v"(dst) : "v"(addr), "i"(off))
#define BLD128(dst, voff, rs, soff) asm volatile("buffer_load_dwordx4 %0, %1, %2, %3 offen" : "=v"(dst) : "v"(voff), "s"(rs), "s"(soff))
#define BLD128_SC1(dst, voff, rs, soff) asm volatile("buffer_load_dwordx4 %0, %1, %2, %3 offen sc1" : "=v"(dst) : "v"(voff), "s"(rs), "s"(soff))
#define BLD32(dst, voff, rs, soff) asm volatile("buffer_load_dword %0, %1, %2, %3 offen" : "=v"(dst) : "v"(voff), "s"(rs), "s"(soff))
#define BLD32_SC1(dst, voff, rs, soff) asm volatile("buffer_load_dword %0, %1, %2, %3 offen sc1" : "=v"(dst) : "v"(voff), "s"(rs), "s"(soff))
DI bf16x8 cat2(u32x2 lo, u32x2 hi) { return __builtin_bit_cast(bf16x8, __builtin_shufflevector(lo, hi, 0, 1, 2, 3)); }
DI bf16x8 asb(u32x4 v) { return __builtin_bit_cast(bf16x8, v); }
#define WAITV(n) asm volatile("s_waitcnt vmcnt(" #n ")" ::: "memory")
#define WAITL0() asm volatile("s_waitcnt lgkmcnt(0)" ::: "memory")
#define BAR() __builtin_amdgcn_s_barrier()
#define SCHED0() __builtin_amdgcn_sched_barrier(0)

#define ST_DECL
#define STAMP(i) do {} while (0)
#define ST_FLUSH(k) do {} while (0)
#define ST_ARG
#define ST_PASS

constexpr int K1_XB = 16384, K1_WB = 28672, K1_STAGE = K1_XB + K1_WB, K1_LDS = 3 * K1_STAGE;

struct K1Frag { u32x4 x[4]; u32x2 wl[7], wh[7]; };
#define K1_TIE(F) "+v"(F.x[0]), "+v"(F.x[1]), "+v"(F.x[2]), "+v"(F.x[3]), "+v"(F.wl[0]), "+v"(F.wl[1]), "+v"(F.wl[2]), "+v"(F.wl[3]), \
  "+v"(F.wl[4]), "+v"(F.wl[5]), "+v"(F.wl[6]), "+v"(F.wh[0]), "+v"(F.wh[1]), "+v"(F.wh[2]), "+v"(F.wh[3]), "+v"(F.wh[4]), "+v"(F.wh[5]), "+v"(F.wh[6])

template <int I, int KS>
DI void k1_rd1(K1Frag& F, unsigned xa, unsigned we, unsigned wo_) {
  if constexpr (I < 4) { DSR128(F.x[I], xa, I * 2048); }
  else if constexpr (I < 18) {
    constexpr int c = (I - 4) >> 1, hi = (I - 4) & 1, o = KS * 32 * 448 + c * 32 + hi * 1792;
    if constexpr (hi) { if constexpr (c & 1) DSRTR(F.wh[c], wo_, o); else DSRTR(F.wh[c], we, o); }
    else { if constexpr (c & 1) DSRTR(F.wl[c], wo_, o); else DSRTR(F.wl[c], we, o); }
  }
}
template <bool ISV, int I>
DI void k1_mm1(f32x4 (&acc)[7][4], const K1Frag& F) {
  constexpr int c = I >> 2, mt = I & 3;
  bf16x8 wf = cat2(F.wl[c], F.wh[c]);
  acc[c][mt] = ISV ? mfma16(asb(F.x[mt]), wf, acc[c][mt]) : mfma16(wf, asb(F.x[mt]), acc[c][mt]);
}
template <bool ISV, int KS, int... Is>
DI void k1_step(f32x4 (&acc)[7][4], const K1Frag& C, K1Frag& N, unsigned xa, unsigned we, unsigned wo_, std::integer_sequence<int, Is...>) {
  ((k1_mm1<ISV, Is>(acc, C), k1_rd1<Is, KS>(N, xa, we, wo_), SCHED0()), ...);
}
template <int KS, int... Is>
DI void k1_rd_all(K1Frag& N, unsigned xa, unsigned we, unsigned wo_, std::integer_sequence<int, Is...>) {
  (k1_rd1<Is, KS>(N, xa, we, wo_), ...);
}

template <bool ISV>
DI void k1_compute(f32x4 (&acc)[7][4], unsigned xa0, unsigned xa1, unsigned wa_e, unsigned wa_o ST_ARG) {
  K1Frag F0, F1;
  BAR();
  STAMP(3);
  k1_rd_all<0>(F0, xa0, wa_e, wa_o, std::make_integer_sequence<int, 18>());
  int buf = 0;
#pragma unroll 1
  for (int s = 0; s < 7; ++s) {
    unsigned bo = buf * K1_STAGE;
    asm volatile("s_waitcnt lgkmcnt(0)" : K1_TIE(F0));
    k1_step<ISV, 1>(acc, F0, F1, xa1 + bo, wa_e + bo, wa_o + bo, std::make_integer_sequence<int, 28>());
    int nb = buf == 2 ? 0 : buf + 1;
    asm volatile("s_waitcnt lgkmcnt(0)" : K1_TIE(F1) :: "memory");
    BAR();
    unsigned bn = nb * K1_STAGE;
    k1_step<ISV, 0>(acc, F1, F0, xa0 + bn, wa_e + bn, wa_o + bn, std::make_integer_sequence<int, 28>());
    buf = nb;
  }
  asm volatile("s_waitcnt lgkmcnt(0)" : K1_TIE(F0));
  STAMP(10);
}

__global__ __launch_bounds__(512, 2) void k1_gemm(const unsigned short* __restrict__ x, const unsigned short* __restrict__ wqkv,
                                                  const unsigned short* __restrict__ w1, const unsigned short* __restrict__ gw,
                                                  const unsigned short* __restrict__ w2, const unsigned short* __restrict__ wo,
                                                  char* __restrict__ ws) {
  extern __shared__ __attribute__((aligned(16))) char smem[];
  const int tid = threadIdx.x, lane = tid & 63;
  const int w = __builtin_amdgcn_readfirstlane(tid >> 6);
  const int xcd = blockIdx.x & 7, idx = blockIdx.x >> 3;
  ST_DECL
  STAMP(0);
  if (idx >= 28) {
    const int gb = xcd * 4 + (idx - 28);
    if (tid < 8) *(unsigned*)(ws + OFF_F + (gb * 8 + tid) * 128) = 0u;
    const int t0 = gb * 64 + w * 8;
    u32x4 gv = {0, 0, 0, 0};
    if (lane < 56) gv = *(const u32x4*)(gw + lane * 8);
    float* g = (float*)(ws + OFF_G);
    u32x4 xv[8];
#pragma unroll
    for (int t = 0; t < 8; ++t) {
      xv[t] = u32x4{0, 0, 0, 0};
      if (lane < 56) xv[t] = *(const u32x4*)(x + (size_t)(t0 + t) * D_ + lane * 8);
    }
    float mine = 0.f;
#pragma unroll
    for (int t = 0; t < 8; ++t) {
      float s = 0.f;
#pragma unroll
      for (int j = 0; j < 4; ++j) {
        s += rbf(__uint_as_float(xv[t][j] << 16) * __uint_as_float(gv[j] << 16));
        s += rbf(__uint_as_float(xv[t][j] & 0xffff0000u) * __uint_as_float(gv[j] & 0xffff0000u));
      }
#pragma unroll
      for (int o = 32; o > 0; o >>= 1) s += __shfl_xor(s, o);
      if (lane == t) mine = s;
    }
    float z = rbf(mine);
    float gg = rbf(1.f / (1.f + __expf(-z)));
    if (lane < 8) g[t0 + lane] = gg;
    STAMP(15);
    ST_FLUSH(0);
    return;
  }
  const int tt = 2 * xcd + (idx >= 14 ? 1 : 0), ct = idx >= 14 ? idx - 14 : idx;
  const bool isq = ct < 6;
  const unsigned lds0 = (unsigned)(uintptr_t)(lds_vptr)smem;
  if (w >= 4) {
    const int lw = w - 4;
    const unsigned strideB = isq ? 2688u : 3584u;
    const unsigned col0B = (isq ? ct : ct - 6) * 448u;
    __amdgpu_buffer_rsrc_t rx = mkrsrc(x, S_ * D_ * 2);
    __amdgpu_buffer_rsrc_t rw = mkrsrc(isq ? wqkv : w1, isq ? D_ * 3 * D_ * 2 : D_ * FF * 2);
    unsigned vx[2], vw[2];
#pragma unroll
    for (int u = 0; u < 2; ++u) vx[u] = (lane >> 3) * 896 + (((lane & 7) ^ (((lane >> 4) + 4 * u) & 7)) << 4);
    { const int hi = lane >= 28 ? 1 : 0, p = lane - 28 * hi;
      vw[0] = hi * strideB + p * 16; vw[1] = hi * strideB + (p ^ 2) * 16; }
    const unsigned sx0 = (unsigned)(128 * tt + 32 * lw) * 896u;
    const unsigned sw0 = col0B + 16 * lw * strideB;
    const unsigned wstep = 64 * strideB;
    auto issue = [&](int s, int buf) {
      const unsigned lb = lds0 + buf * K1_STAGE;
#pragma unroll
      for (int u = 0; u < 4; ++u) glds16(rx, lb + (4 * lw + u) * 1024, vx[u & 1], sx0 + s * 128 + u * (8 * 896));
      if (lane < 56) {
#pragma unroll
        for (int u = 0; u < 8; ++u) glds16(rw, lb + K1_XB + (8 * lw + u) * 896, vw[(u >> 2) & 1], sw0 + s * wstep + 2 * u * strideB);
      }
    };
    issue(0, 0); issue(1, 1);
    STAMP(1);
    WAITV(12); BAR();
    STAMP(2);
    issue(2, 2);
    int buf = 0;
#pragma unroll 1
    for (int s = 0; s < 4; ++s) {
      WAITV(12);
      BAR();
      issue(s + 3, buf);
      buf = buf == 2 ? 0 : buf + 1;
    }
    WAITV(12);
    BAR();
    { const unsigned pb = lds0 + 1 * K1_STAGE + lw * 11264;
      const __amdgpu_buffer_rsrc_t r2 = mkrsrc(w2, FF * D_ * 2), ro = mkrsrc(wo, D_ * D_ * 2);
      const int slice = idx * 4 + lw;
#pragma unroll
      for (int u = 0; u < 14; ++u) glds16(r2, pb + (u % 11) * 1024, lane * 16, (slice * 14 + u) * 1024);
#pragma unroll
      for (int u = 0; u < 4; ++u) glds16(ro, pb + u * 1024, lane * 16, slice * 3584 + u * 1024);
    }
    WAITV(18);
    BAR();
    BAR();
    ST_FLUSH(0);
    return;
  }
  const int wm = w & 1, wn = w >> 1;
  const int i16 = lane & 15, g = lane >> 4;
  const unsigned xrow = (64 * wm + i16) * 128;
  const unsigned xa0 = lds0 + xrow + (((g) ^ (i16 >> 1)) << 4);
  const unsigned xa1 = lds0 + xrow + (((4 + g) ^ (i16 >> 1)) << 4);
  const unsigned wbase = lds0 + K1_XB + (8 * g + (i16 >> 2)) * 448 + 8 * (i16 & 3) + 7 * wn * 32;
  const int b = g & 1;
  const unsigned wa_e = wbase + ((wn & 1) ? -32 * b : 32 * b);
  const unsigned wa_o = wbase + ((wn & 1) ? 32 * b : -32 * b);
  f32x4 acc[7][4];
#pragma unroll
  for (int c = 0; c < 7; ++c)
#pragma unroll
    for (int m = 0; m < 4; ++m) acc[c][m] = f32x4{0.f, 0.f, 0.f, 0.f};
  const bool isv = (ct == 4 || ct == 5);
  if (isv) k1_compute<true>(acc, xa0, xa1, wa_e, wa_o ST_PASS);
  else k1_compute<false>(acc, xa0, xa1, wa_e, wa_o ST_PASS);

  const int t16 = 8 * tt + 4 * wm;
  const __amdgpu_buffer_rsrc_t rws = mkrsrc(ws, WS_BYTES);
#define ST16(p, v) __builtin_amdgcn_raw_buffer_store_b128((v), rws, (unsigned)((char*)(p) - ws), 0, 16)
#define ST8(p, v) __builtin_amdgcn_raw_buffer_store_b64((v), rws, (unsigned)((char*)(p) - ws), 0, 16)
  if (ct >= 6) {
    char* hb = ws + OFF_H + lane * 16;
    const int m0 = 7 * (ct - 6);
#pragma unroll
    for (int mt = 0; mt < 4; ++mt) {
      unsigned p[7][2];
#pragma unroll
      for (int c = 0; c < 7; ++c) {
        p[c][0] = relu2(pk2(acc[c][mt][0], acc[c][mt][1]));
        p[c][1] = relu2(pk2(acc[c][mt][2], acc[c][mt][3]));
      }
      char* hp = hb + (size_t)((t16 + mt) * 56 + m0) * 1024;
      if (wn == 0) {
#pragma unroll
        for (int q = 0; q < 3; ++q) ST16(hp + q * 1024, (u32x4{p[2 * q][0], p[2 * q][1], p[2 * q + 1][0], p[2 * q + 1][1]}));
        ST8(hp + 3 * 1024, (u32x2{p[6][0], p[6][1]}));
      } else {
        ST8(hp + 3 * 1024 + 8, (u32x2{p[0][0], p[0][1]}));
#pragma unroll
        for (int q = 0; q < 3; ++q) ST16(hp + (4 + q) * 1024, (u32x4{p[2 * q + 1][0], p[2 * q + 1][1], p[2 * q + 2][0], p[2 * q + 2][1]}));
      }
    }
  } else if (!isv) {
    const int h0 = (ct & 1) * 8 + wn * 4;
    char* qb = ws + (ct < 2 ? OFF_Q : OFF_K) + lane * 16;
#pragma unroll
    for (int mt = 0; mt < 4; ++mt) {
      unsigned p[7][2];
#pragma unroll
      for (int c = 0; c < 7; ++c) {
        p[c][0] = pk2(acc[c][mt][0], acc[c][mt][1]);
        p[c][1] = pk2(acc[c][mt][2], acc[c][mt][3]);
      }
      u32x4 f0 = {p[0][0], p[0][1], g < 3 ? p[1][0] : 0u, g < 3 ? p[1][1] : 0u};
      u32x4 f1 = {p[2][0], p[2][1], g < 2 ? p[3][0] : (g == 2 ? 0u : p[1][0]), g < 2 ? p[3][1] : (g == 2 ? 0u : p[1][1])};
      u32x4 f2 = {p[4][0], p[4][1], g == 0 ? p[5][0] : (g == 1 ? 0u : p[3][0]), g == 0 ? p[5][1] : (g == 1 ? 0u : p[3][1])};
      u32x4 f3 = {p[6][0], p[6][1], g == 0 ? 0u : p[5][0], g == 0 ? 0u : p[5][1]};
      char* qp = qb + (size_t)(h0 * 128 + t16 + mt) * 1024;
      ST16(qp, (f0));
      ST16(qp + 128 * 1024, (f1));
      ST16(qp + 2 * 128 * 1024, (f2));
      ST16(qp + 3 * 128 * 1024, (f3));
    }
  } else {
    const int h0 = (ct & 1) * 8 + wn * 4;
    const int m32 = 4 * tt + 2 * wm;
    char* vb = ws + OFF_V;
#pragma unroll
    for (int c = 0; c < 7; ++c) {
      int col = 16 * c + i16;
      int hh = col / 28, d = col - hh * 28;
      char* vp = vb + (size_t)(((h0 + hh) * 64 + m32) * 2 + (d >> 4)) * 1024 + (g * 16 + (d & 15)) * 16;
#pragma unroll
      for (int q = 0; q < 2; ++q) {
        u32x4 f = {pk2(acc[c][2 * q][0], acc[c][2 * q][1]), pk2(acc[c][2 * q][2], acc[c][2 * q][3]),
                   pk2(acc[c][2 * q + 1][0], acc[c][2 * q + 1][1]), pk2(acc[c][2 * q + 1][2], acc[c][2 * q + 1][3])};
        ST16(vp + q * 2048, (f));
      }
    }
    if (i16 < 8) {
      int hh = i16 & 3, q = i16 >> 2;
      char* vp = vb + (size_t)(((h0 + hh) * 64 + m32 + q) * 2 + 1) * 1024 + (g * 16 + 12) * 16;
      ST16(vp, (u32x4{0x3f803f80u, 0x3f803f80u, 0x3f803f80u, 0x3f803f80u}));
    }
  }
  STAMP(11);
  STAMP(12);
  ST_FLUSH(0);
}

constexpr int K23_RING = 49152, K23_LDS = K23_RING + 8 * 3 * 4096;

DI float xmax16_32(float v) {
  auto a = __builtin_amdgcn_permlane16_swap(__float_as_uint(v), __float_as_uint(v), false, false);
  v = fmaxf(__uint_as_float(a[0]), __uint_as_float(a[1]));
  auto b = __builtin_amdgcn_permlane32_swap(__float_as_uint(v), __float_as_uint(v), false, false);
  return fmaxf(__uint_as_float(b[0]), __uint_as_float(b[1]));
}
struct BFrag { u32x4 b[4]; };
#define B_TIE(B) "+v"(B.b[0]), "+v"(B.b[1]), "+v"(B.b[2]), "+v"(B.b[3])

struct K3Ctx {
  __amdgpu_buffer_rsrc_t rs, r2, ro;
  unsigned ldw;
  unsigned va, va3o, vl;
  unsigned sA, sB;
  unsigned sAo, sBo;
  unsigned ta[4];
};
template <int I>
DI void k3_issueA(const K3Ctx& c) {
  constexpr int slot = I % 3;
  if constexpr (I < 7) {
#pragma unroll
    for (int u = 0; u < 4; ++u) glds16(c.r2, c.ldw + slot * 4096 + u * 1024, c.va, c.sA + (32 * I + 8 * u) * 896);
  } else {
#pragma unroll
    for (int u = 0; u < 4; ++u) glds16(c.ro, c.ldw + slot * 4096 + u * 1024, u == 3 ? c.va3o : c.va, c.sAo + (28 * (I - 7) + 8 * u) * 896);
  }
}
template <int I>
DI void k3_issueB(const K3Ctx& c, BFrag& B) {
  if constexpr (I < 7) {
    BLD128(B.b[0], c.vl, c.rs, c.sB + I * 1024);
    BLD128(B.b[1], c.vl, c.rs, c.sB + I * 1024 + 56 * 1024);
    BLD128(B.b[2], c.vl, c.rs, c.sB + I * 1024 + 2 * 56 * 1024);
    BLD128(B.b[3], c.vl, c.rs, c.sB + I * 1024 + 3 * 56 * 1024);
  } else {
    BLD128_SC1(B.b[0], c.vl, c.rs, c.sBo + (I - 7) * 1024);
    BLD128_SC1(B.b[1], c.vl, c.rs, c.sBo + (I - 7) * 1024 + 16 * 1024);
    BLD128_SC1(B.b[2], c.vl, c.rs, c.sBo + (I - 7) * 1024 + 2 * 16 * 1024);
    BLD128_SC1(B.b[3], c.vl, c.rs, c.sBo + (I - 7) * 1024 + 3 * 16 * 1024);
  }
}
template <int I, bool REFILL>
DI void k3_step(const K3Ctx& c, f32x4 (&acc)[4][4], BFrag& B) {
  constexpr int so = (I % 3) * 4096;
  u32x2 al[4], ah[4];
  DSRTR(al[0], c.ta[0], so); DSRTR(ah[0], c.ta[0], so + 2048); DSRTR(al[1], c.ta[1], so); DSRTR(ah[1], c.ta[1], so + 2048);
  DSRTR(al[2], c.ta[2], so); DSRTR(ah[2], c.ta[2], so + 2048); DSRTR(al[3], c.ta[3], so); DSRTR(ah[3], c.ta[3], so + 2048);
  asm volatile("s_waitcnt lgkmcnt(0)" : "+v"(al[0]), "+v"(al[1]), "+v"(al[2]), "+v"(al[3]), "+v"(ah[0]), "+v"(ah[1]), "+v"(ah[2]), "+v"(ah[3]) :: "memory");
  if constexpr (REFILL) k3_issueA<I + 3>(c);
  SCHED0();
#pragma unroll
  for (int cc = 0; cc < 4; ++cc)
#pragma unroll
    for (int tk = 0; tk < 4; ++tk) acc[cc][tk] = mfma16(cat2(al[cc], ah[cc]), asb(B.b[tk]), acc[cc][tk]);
  SCHED0();
}

__global__ __launch_bounds__(512, 2) void k23(const unsigned short* __restrict__ w2, const unsigned short* __restrict__ wo,
                                              char* __restrict__ ws, unsigned short* __restrict__ out) {
  extern __shared__ __attribute__((aligned(16))) char smem[];
  const int tid = threadIdx.x, lane = tid & 63;
  const int w = __builtin_amdgcn_readfirstlane(tid >> 6);
  const int xcd = blockIdx.x & 7, idx = blockIdx.x >> 3;
  const int qb = 2 * xcd + (idx >> 4), h = idx & 15;
  const bool has3 = idx < 28;
  const int t64 = 4 * xcd + (has3 ? idx / 7 : 0), nt = has3 ? idx % 7 : 0;
  ST_DECL
  STAMP(0);
  const unsigned lds0 = (unsigned)(uintptr_t)(lds_vptr)smem;
  const int i16 = lane & 15, g = lane >> 4;
  K3Ctx c;
  c.rs = mkrsrc(ws, WS_BYTES);
  c.vl = lane * 16;
#pragma unroll
  for (int u = 0; u < 3; ++u) {
    int i = 3 * w + u;
    int U = 8 * qb - 16 + i; U = U < 0 ? 0 : U;
    glds16(c.rs, lds0 + i * 1024, c.vl, OFF_K + (h * 128 + U) * 1024);
  }
#pragma unroll
  for (int u = 0; u < 3; ++u) {
    int i = 3 * w + u;
    int m = 4 * qb - 8 + (i >> 1); m = m < 0 ? 0 : m;
    glds16(c.rs, lds0 + 24576 + i * 1024, c.vl, OFF_V + ((h * 64 + m) * 2 + (i & 1)) * 1024);
  }
  const int T = 8 * qb + w;
  u32x4 qf; unsigned gq, g3[4];
  BLD128(qf, c.vl, c.rs, OFF_Q + (h * 128 + T) * 1024);
  { unsigned vg = i16 * 4;
    BLD32(gq, vg, c.rs, OFF_G + T * 64);
    BLD32(g3[0], vg, c.rs, OFF_G + t64 * 256); BLD32(g3[1], vg, c.rs, OFF_G + t64 * 256 + 64);
    BLD32(g3[2], vg, c.rs, OFF_G + t64 * 256 + 128); BLD32(g3[3], vg, c.rs, OFF_G + t64 * 256 + 192); }
  c.r2 = mkrsrc(w2, FF * D_ * 2);
  c.ro = mkrsrc(wo, D_ * D_ * 2);
  c.ldw = lds0 + K23_RING + w * (3 * 4096);
  { const int r8 = lane >> 3;
    const unsigned vchunk = (unsigned)(((lane & 7) ^ (2 * ((r8 >> 1) & 3))) << 4) + nt * 128;
    c.va = r8 * 896 + vchunk;
    c.va3o = (r8 < 4 ? r8 : 3) * 896 + vchunk; }
  c.sA = (unsigned)(7 * w) * 32 * 896;
  c.sB = OFF_H + (unsigned)((4 * t64) * 56 + 7 * w) * 1024;
  c.sAo = (unsigned)(2 * w) * 28 * 896;
  c.sBo = OFF_O + (unsigned)((4 * t64) * 16 + 2 * w) * 1024;
  BFrag B0, B1, B2;
  STAMP(1);
  asm volatile("s_waitcnt vmcnt(0)" : "+v"(qf), "+v"(gq), "+v"(g3[0]), "+v"(g3[1]), "+v"(g3[2]), "+v"(g3[3]) :: "memory");
  BAR();
  STAMP(2);
  if (has3) { k3_issueA<0>(c); k3_issueB<0>(c, B0); }
  {
    const float NEG = -1e30f;
    const bool odd = w & 1;
    f32x4 c0, c1, c16, c17;
#pragma unroll
    for (int r = 0; r < 4; ++r) {
      float lo = (4 * g + r > i16) ? 0.f : NEG, hi = (4 * g + r <= i16) ? 0.f : NEG;
      c0[r] = odd ? NEG : lo; c1[r] = odd ? lo : 0.f; c16[r] = odd ? 0.f : hi; c17[r] = odd ? hi : NEG;
    }
    const f32x4 z4 = {0.f, 0.f, 0.f, 0.f};
    f32x4 s[18];
    const char* kp = smem + (w & ~1) * 1024 + lane * 16;
    const bf16x8 q = asb(qf);
#pragma unroll
    for (int i = 0; i < 18; ++i) {
      bf16x8 kf = *(const bf16x8*)(kp + i * 1024);
      s[i] = mfma16(kf, q, i == 0 ? c0 : (i == 1 ? c1 : (i == 16 ? c16 : (i == 17 ? c17 : z4))));
    }
    if (qb < 2) {
      const int first = 16 - 8 * qb - (w & ~1);
#pragma unroll
      for (int i = 0; i < 16; ++i)
        if (i < first) s[i] = f32x4{NEG, NEG, NEG, NEG};
    }
    STAMP(3);
    if (has3) { k3_issueA<1>(c); k3_issueB<1>(c, B1); }
    float m0 = NEG;
#pragma unroll
    for (int i = 0; i < 18; ++i) { m0 = fmaxf(fmaxf(m0, s[i][0]), s[i][1]); m0 = fmaxf(fmaxf(m0, s[i][2]), s[i][3]); }
    m0 = xmax16_32(m0);
    const float cs = 0.18898223650461357f * 1.4426950408889634f;
    const float n0 = -m0 * cs;
    f32x4 o[2] = {z4, z4};
    const char* vp = smem + 24576 + (w & ~1) * 1024 + lane * 16;
#pragma unroll
    for (int m = 0; m < 9; ++m) {
      u32x4 p;
#pragma unroll
      for (int t = 0; t < 2; ++t) {
        const f32x4 v = s[2 * m + t];
        p[2 * t] = pk2(__builtin_amdgcn_exp2f(fmaf(v[0], cs, n0)), __builtin_amdgcn_exp2f(fmaf(v[1], cs, n0)));
        p[2 * t + 1] = pk2(__builtin_amdgcn_exp2f(fmaf(v[2], cs, n0)), __builtin_amdgcn_exp2f(fmaf(v[3], cs, n0)));
      }
#pragma unroll
      for (int dt = 0; dt < 2; ++dt) o[dt] = mfma16(*(const bf16x8*)(vp + (2 * m + dt) * 1024), asb(p), o[dt]);
    }
    STAMP(4);
    float l = __shfl(o[1][0], 48 + i16);
    float f = __uint_as_float(gq) / l;
    u32x4 r = {pk2(o[0][0] * f, o[0][1] * f), pk2(o[0][2] * f, o[0][3] * f), pk2(o[1][0] * f, o[1][1] * f), pk2(o[1][2] * f, o[1][3] * f)};
    if (g == 3) { r[2] = 0u; r[3] = 0u; }
    __builtin_amdgcn_raw_buffer_store_b128(r, c.rs, c.vl, OFF_O + (T * 16 + h) * 1024, 16);
  }
  WAITV(0);
  BAR();
  if (tid == 0) __hip_atomic_store((gu32*)(ws + OFF_F + (qb * 16 + h) * 128), 1u, __ATOMIC_RELAXED, __HIP_MEMORY_SCOPE_AGENT);
  STAMP(5);
  if (!has3) { ST_FLUSH(1); return; }
  f32x4 acc[4][4];
#pragma unroll
  for (int cc = 0; cc < 4; ++cc)
#pragma unroll
    for (int tk = 0; tk < 4; ++tk) acc[cc][tk] = f32x4{0.f, 0.f, 0.f, 0.f};
  { const int trow = 4 * g + (i16 >> 2);
#pragma unroll
    for (int cc = 0; cc < 4; ++cc) c.ta[cc] = c.ldw + trow * 128 + ((cc ^ ((trow >> 1) & 3)) * 32) + 8 * (i16 & 3); }
  k3_issueA<2>(c); k3_issueB<2>(c, B2);
  asm volatile("" : B_TIE(B0), B_TIE(B1));
  k3_step<0, true>(c, acc, B0); k3_issueB<3>(c, B0);
  k3_step<1, true>(c, acc, B1); k3_issueB<4>(c, B1);
  asm volatile("s_waitcnt vmcnt(16)" : B_TIE(B2) :: "memory");
  k3_step<2, true>(c, acc, B2); k3_issueB<5>(c, B2);
  unsigned fl;
  const unsigned vf = (lane & 1) * 128, sf = OFF_F + (unsigned)((t64 >> 1) * 16 + 2 * w) * 128;
  BLD32_SC1(fl, vf, c.rs, sf);
  STAMP(6);
  asm volatile("s_waitcnt vmcnt(17)" : B_TIE(B0) :: "memory");
  k3_step<3, true>(c, acc, B0); k3_issueB<6>(c, B0);
  asm volatile("s_waitcnt vmcnt(17)" : B_TIE(B1) :: "memory");
  k3_step<4, true>(c, acc, B1);
  asm volatile("s_waitcnt vmcnt(12)" : "+v"(fl), B_TIE(B2) :: "memory");
  for (unsigned spins = 0; !__all(fl != 0u) && spins < (1u << 22); ++spins) {
    __builtin_amdgcn_s_sleep(1);
    BLD32_SC1(fl, vf, c.rs, sf);
    asm volatile("s_waitcnt vmcnt(0)" : "+v"(fl) :: "memory");
  }
  k3_issueB<7>(c, B1);
  k3_step<5, true>(c, acc, B2); k3_issueB<8>(c, B2);
  asm volatile("s_waitcnt vmcnt(16)" : B_TIE(B0) :: "memory");
  k3_step<6, false>(c, acc, B0);
  STAMP(7);
#pragma unroll
  for (int tk = 0; tk < 4; ++tk) {
    float omg = rbf(1.f - __uint_as_float(g3[tk]));
#pragma unroll
    for (int cc = 0; cc < 4; ++cc) acc[cc][tk] *= omg;
  }
  asm volatile("s_waitcnt vmcnt(8)" : B_TIE(B1) :: "memory");
  k3_step<7, false>(c, acc, B1);
  asm volatile("s_waitcnt vmcnt(0)" : B_TIE(B2) :: "memory");
  k3_step<8, false>(c, acc, B2);
  STAMP(8);
  BAR();
#pragma unroll
  for (int cc = 0; cc < 4; ++cc)
#pragma unroll
    for (int tk = 0; tk < 4; ++tk) *(lds_f32x4p)(uintptr_t)(lds0 + w * 16384 + (tk * 4 + cc) * 1024 + lane * 16) = acc[cc][tk];
  __syncthreads();
  STAMP(9);
  const unsigned ob = lds0 + 131072;
#pragma unroll
  for (int j = 0; j < 2; ++j) {
    const int tile = 2 * w + j, tk = tile >> 2, cc = tile & 3;
    f32x4 s = *(lds_f32x4p)(uintptr_t)(lds0 + tile * 1024 + lane * 16);
#pragma unroll
    for (int ww = 1; ww < 8; ++ww) s += *(lds_f32x4p)(uintptr_t)(lds0 + ww * 16384 + tile * 1024 + lane * 16);
    const int row = 16 * tk + i16;
    unsigned a = ob + row * 128 + (((2 * cc + (g >> 1)) ^ (row & 7)) << 4) + (g & 1) * 8;
    *(lds_u32x2p)(uintptr_t)a = u32x2{pk2(s[0], s[1]), pk2(s[2], s[3])};
  }
  __syncthreads();
  { const int row = 8 * w + (lane >> 3), ch = lane & 7;
    u32x4 v = *(lds_u32x4p)(uintptr_t)(ob + row * 128 + ((ch ^ (row & 7)) << 4));
    *(u32x4*)((char*)out + (size_t)(64 * t64 + row) * 896 + nt * 128 + ch * 16) = v; }
  STAMP(10);
  STAMP(11);
  ST_FLUSH(1);
}

extern "C" void kernel_launch(void* const* d_in, const int* in_sizes, int n_in, void* d_out, int out_size, void* d_ws,
                              size_t ws_size, hipStream_t stream) {
  const unsigned short* x = (const unsigned short*)d_in[0];
  const unsigned short* wqkv = (const unsigned short*)d_in[1];
  const unsigned short* wo = (const unsigned short*)d_in[2];
  const unsigned short* w1 = (const unsigned short*)d_in[4];
  const unsigned short* w2 = (const unsigned short*)d_in[5];
  const unsigned short* gw = (const unsigned short*)d_in[6];
  char* ws = (char*)d_ws;
  (void)hipFuncSetAttribute((const void*)k1_gemm, hipFuncAttributeMaxDynamicSharedMemorySize, K1_LDS);
  (void)hipFuncSetAttribute((const void*)k23, hipFuncAttributeMaxDynamicSharedMemorySize, K23_LDS);
  k1_gemm<<<256, 512, K1_LDS, stream>>>(x, wqkv, w1, gw, w2, wo, ws);
  k23<<<256, 512, K23_LDS, stream>>>(w2, wo, ws, (unsigned short*)d_out);
}
```

```cpp
#include <hip/hip_runtime.h>
#include <stdint.h>
#include <utility>

typedef __attribute__((ext_vector_type(8))) __bf16 bf16x8;
typedef __attribute__((ext_vector_type(2))) __bf16 bf16x2;
typedef __attribute__((ext_vector_type(2))) float f32x2;
typedef __attribute__((ext_vector_type(4))) float f32x4;
typedef __attribute__((ext_vector_type(2))) short s16x2;
typedef __attribute__((ext_vector_type(4))) unsigned u32x4;
typedef __attribute__((ext_vector_type(2))) unsigned u32x2;
typedef __attribute__((address_space(3))) void* lds_vptr;
typedef __attribute__((address_space(3))) unsigned* lds_u32p;
typedef __attribute__((address_space(3))) u32x4* lds_u32x4p;
typedef __attribute__((address_space(3))) u32x2* lds_u32x2p;
typedef __attribute__((address_space(3))) f32x4* lds_f32x4p;
typedef __attribute__((address_space(1))) unsigned long long gu64;
#define DI __device__ __forceinline__

#define PF_W2 0
#define PF_CODE 1
#define H_DELAY 0
#define NSLOT 6
#define B_AFTER_KV 0
#define PF_MALL 1
#define PF_TLB 1
#define V_LDS 1
#define ATT_PRIO 1
#define NPRE 6
#define PF_W2_DELAY 0
#define PF_BURST 2
#define OUT_AUX -1
#define BURST_DELAY 10
#define H_PUB_LATE 0
#define NEARLY 6
#define P1_PRIO 0
#define ATT_BY_HEAD 1
#define PUB_AT 3
#define CODE_KB 28

constexpr int S_ = 2048, D_ = 448, FF = 1792;
constexpr unsigned OFF_Q = 0u, OFF_K = 2u << 20, OFF_V = 4u << 20, OFF_O = 6u << 20, OFF_H = 8u << 20, OFF_G = 16u << 20, OFF_F = (16u << 20) + 65536u;
constexpr unsigned WS_BYTES = 17u << 20;

extern "C" __device__ unsigned long long hw_dispatch_id() __asm("llvm.amdgcn.dispatch.id");

DI unsigned pk2(float a, float b) {
  f32x2 f = {a, b};
  return __builtin_bit_cast(unsigned, __builtin_convertvector(f, bf16x2));
}
DI unsigned relu2(unsigned p) { return __builtin_bit_cast(unsigned, __builtin_elementwise_max(__builtin_bit_cast(s16x2, p), s16x2{0, 0})); }
DI float rbf(float x) { return __uint_as_float(pk2(x, 0.f) << 16); }
DI f32x4 mfma16(bf16x8 a, bf16x8 b, f32x4 c) { return __builtin_amdgcn_mfma_f32_16x16x32_bf16(a, b, c, 0, 0, 0); }
DI __amdgpu_buffer_rsrc_t mkrsrc(const void* p, unsigned bytes) { return __builtin_amdgcn_make_buffer_rsrc((void*)p, 0, bytes, 0x00020000); }
DI void glds16(__amdgpu_buffer_rsrc_t rs, unsigned lds_addr, unsigned voff, unsigned soff) {
  __builtin_amdgcn_raw_ptr_buffer_load_lds(rs, (lds_vptr)(uintptr_t)lds_addr, 16, voff, soff, 0, 0);
}
DI void glds16_sc1(__amdgpu_buffer_rsrc_t rs, unsigned lds_addr, unsigned voff, unsigned soff) {
  __builtin_amdgcn_raw_ptr_buffer_load_lds(rs, (lds_vptr)(uintptr_t)lds_addr, 16, voff, soff, 0, 16);
}
#define DSR128(dst, addr, off) asm volatile("ds_read_b128 %0, %1 offset:%2" : "=v"(dst) : "v"(addr), "i"(off))
#define DSRTR(dst, addr, off) asm volatile("ds_read_b64_tr_b16 %0, %1 offset:%2" : "=v"(dst) : "v"(addr), "i"(off))
#define BLD128_SC1(dst, voff, rs, soff) asm volatile("buffer_load_dwordx4 %0, %1, %2, %3 offen sc1" : "=v"(dst) : "v"(voff), "s"(rs), "s"(soff))
#define BLD64_SC1(dst, voff, rs, soff) asm volatile("buffer_load_dwordx2 %0, %1, %2, %3 offen sc1" : "=v"(dst) : "v"(voff), "s"(rs), "s"(soff))
#define BLD32_SC1(dst, voff, rs, soff) asm volatile("buffer_load_dword %0, %1, %2, %3 offen sc1" : "=v"(dst) : "v"(voff), "s"(rs), "s"(soff))
DI bf16x8 cat2(u32x2 lo, u32x2 hi) { return __builtin_bit_cast(bf16x8, __builtin_shufflevector(lo, hi, 0, 1, 2, 3)); }
DI bf16x8 asb(u32x4 v) { return __builtin_bit_cast(bf16x8, v); }
#define WAITV(n) asm volatile("s_waitcnt vmcnt(" #n ")" ::: "memory")
#define WAITL0() asm volatile("s_waitcnt lgkmcnt(0)" ::: "memory")
#define BAR() __builtin_amdgcn_s_barrier()
#define SCHED0() __builtin_amdgcn_sched_barrier(0)
#define ST16(rs, off, v) __builtin_amdgcn_raw_buffer_store_b128((v), (rs), (off), 0, 16)
#define ST8(rs, off, v) __builtin_amdgcn_raw_buffer_store_b64((v), (rs), (off), 0, 16)

#define ST_DECL
#define STAMP(i) do {} while (0)
#define ST_FLUSH(k) do {} while (0)
#define ST_ARG
#define ST_PASS

constexpr int L1_X = 0, L1_W = 7 * 16384, LDS_TOTAL = L1_W + 3 * 16384;
constexpr int ring_addr(int q) { return q < 3 ? L1_W + ((q + 1) % 3) * 16384 : L1_X + (q + 1) * 16384; }
constexpr int LDS_K = 0, LDS_V = 24576, LDS_Q = 49152, LDS_G = 57344, LDS_SYNC = 57856;
static_assert(NSLOT == 6 && NPRE == 6, "map");
constexpr unsigned F_QKV = 0, F_H = 16, F_G = 512, F_O = 576;

DI void wait_tags(__amdgpu_buffer_rsrc_t rs, unsigned voff, unsigned long long tag) {
  for (unsigned spins = 0; spins < (1u << 20); ++spins) {
    u32x2 v;
    BLD64_SC1(v, voff, rs, OFF_F);
    asm volatile("s_waitcnt vmcnt(0)" : "+v"(v) :: "memory");
    if (__all(v[0] == (unsigned)tag && v[1] == (unsigned)(tag >> 32))) break;
    __builtin_amdgcn_s_sleep(2);
  }
}
DI void set_flag(char* ws, unsigned slot, unsigned long long tag) {
  __hip_atomic_store((gu64*)(ws + OFF_F + slot * 128), tag, __ATOMIC_RELAXED, __HIP_MEMORY_SCOPE_AGENT);
}
DI void lds_mark(unsigned base, int id, unsigned tg, int lane) {
  if (lane == 0) __hip_atomic_store((lds_u32p)(uintptr_t)(base + id * 4), tg, __ATOMIC_RELAXED, __HIP_MEMORY_SCOPE_WORKGROUP);
}
DI void lds_wait4(unsigned base, unsigned tg, int lane) {
  for (unsigned spins = 0; spins < (1u << 22); ++spins) {
    unsigned v = __hip_atomic_load((lds_u32p)(uintptr_t)(base + (lane & 3) * 4), __ATOMIC_RELAXED, __HIP_MEMORY_SCOPE_WORKGROUP);
    if (__all(v == tg)) break;
    __builtin_amdgcn_s_sleep(1);
  }
  asm volatile("" ::: "memory");
}

struct P1Frag { u32x4 x[4]; u32x2 wl[4], wh[4]; };
#define P1_TIE(F) "+v"(F.x[0]), "+v"(F.x[1]), "+v"(F.x[2]), "+v"(F.x[3]), "+v"(F.wl[0]), "+v"(F.wl[1]), "+v"(F.wl[2]), "+v"(F.wl[3]), \
  "+v"(F.wh[0]), "+v"(F.wh[1]), "+v"(F.wh[2]), "+v"(F.wh[3])
template <int R, int KS>
DI void p1_rd1(P1Frag& F, unsigned xa, const unsigned (&wa)[4]) {
  if constexpr (R < 4) { DSR128(F.x[R], xa, R * 2048); }
  else if constexpr (R < 12) {
    constexpr int c = (R - 4) >> 1, hi = (R - 4) & 1, o = KS * 8192 + hi * 1024;
    if constexpr (hi) DSRTR(F.wh[c], wa[c], o); else DSRTR(F.wl[c], wa[c], o);
  }
}
template <bool ISV, int I>
DI void p1_mm1(f32x4 (&acc)[4][4], const P1Frag& F) {
  constexpr int c = I >> 2, mt = I & 3;
  bf16x8 wf = cat2(F.wl[c], F.wh[c]);
  acc[c][mt] = ISV ? mfma16(asb(F.x[mt]), wf, acc[c][mt]) : mfma16(wf, asb(F.x[mt]), acc[c][mt]);
}
#define P1_WAITF(n, F) asm volatile("s_waitcnt lgkmcnt(" #n ")" : P1_TIE(F))
struct P1Next { unsigned loc, dlt, xan; };
DI unsigned p1_wloc(unsigned tbl, int s) {
  return (((tbl >> (4 * s)) & 15u) - 7u) << 14;
}
constexpr unsigned P1_LOC_FIRST = 0x7987987u;
constexpr unsigned P1_LOC_SECOND = 0x3210798u;
template <bool ISV, int I>
DI void p1_slotA(f32x4 (&acc)[4][4], P1Frag& C, P1Frag& N, unsigned xa, unsigned (&wa)[4], unsigned second, int s, unsigned xa0, P1Next& nx) {
  if constexpr (I == 0) P1_WAITF(6, C);
  if constexpr (I == 4) P1_WAITF(8, C);
  if constexpr (I == 8) P1_WAITF(10, C);
  if constexpr (I == 12) P1_WAITF(12, C);
  p1_mm1<ISV, I>(acc, C);
  p1_rd1<I, 1>(N, xa, wa);
  if constexpr (I == 2) {
    const int sn = s < 6 ? s + 1 : 6;
    const unsigned nloc = p1_wloc(second, sn);
    nx.dlt = nloc - nx.loc; nx.loc = nloc; nx.xan = xa0 + sn * 16384;
    asm volatile("" : "+s"(nx.dlt), "+s"(nx.loc), "+v"(nx.xan));
  }
  if constexpr (I >= 12) { wa[I - 12] += nx.dlt; asm volatile("" : "+v"(wa[I - 12])); }
  SCHED0();
}
template <bool ISV, int... Is>
DI void p1_stepA(f32x4 (&acc)[4][4], P1Frag& C, P1Frag& N, unsigned xa, unsigned (&wa)[4], unsigned second, int s, unsigned xa0, P1Next& nx,
                 std::integer_sequence<int, Is...>) {
  (p1_slotA<ISV, Is>(acc, C, N, xa, wa, second, s, xa0, nx), ...);
}
template <bool ISV, int I>
DI void p1_slotB(f32x4 (&acc)[4][4], P1Frag& C, P1Frag& N, unsigned xa, const unsigned (&wa)[4]) {
  p1_mm1<ISV, I>(acc, C);
  if constexpr (I >= 4) p1_rd1<I - 4, 0>(N, xa, wa);
  SCHED0();
}
template <bool ISV, int OFS, int... Is>
DI void p1_stepB(f32x4 (&acc)[4][4], P1Frag& C, P1Frag& N, unsigned xa, const unsigned (&wa)[4], std::integer_sequence<int, Is...>) {
  (p1_slotB<ISV, OFS + Is>(acc, C, N, xa, wa), ...);
}
template <int KS, int... Is>
DI void p1_rd_all(P1Frag& N, unsigned xa, const unsigned (&wa)[4], std::integer_sequence<int, Is...>) {
  (p1_rd1<Is, KS>(N, xa, wa), ...);
}
template <bool ISV>
DI void p1_piece(f32x4 (&acc)[4][4], unsigned xa0, unsigned xa1, const unsigned (&wa0)[4], unsigned second, int pub_at, bool pub, char* ws, unsigned slot_flag,
                 unsigned long long tag, int tid) {
  P1Frag F0, F1;
  unsigned wa[4];
  P1Next nx;
  nx.loc = p1_wloc(second, 0);
#pragma unroll
  for (int c = 0; c < 4; ++c) wa[c] = wa0[c] + nx.loc;
  p1_rd_all<0>(F0, xa0, wa, std::make_integer_sequence<int, 12>());
  unsigned xs = xa1;
#pragma unroll 1
  for (int s = 0; s < 7; ++s) {
    p1_stepA<ISV>(acc, F0, F1, xs, wa, second, s, xa0, nx, std::make_integer_sequence<int, 16>());
    P1_WAITF(6, F1);
    p1_stepB<ISV, 0>(acc, F1, F0, xa0, wa, std::make_integer_sequence<int, 4>());
    asm volatile("s_waitcnt lgkmcnt(0)" : P1_TIE(F1) :: "memory");
    if (s == pub_at) {
      WAITV(0);
      BAR();
      if (pub && tid == 0) set_flag(ws, slot_flag, tag);
    } else {
      BAR();
    }
    xs += 16384;
    p1_stepB<ISV, 4>(acc, F1, F0, nx.xan, wa, std::make_integer_sequence<int, 12>());
  }
  asm volatile("s_waitcnt lgkmcnt(0)" : P1_TIE(F0));
}

struct BFrag { u32x4 b[4]; };
#define B_TIE(B) "+v"(B.b[0]), "+v"(B.b[1]), "+v"(B.b[2]), "+v"(B.b[3])
struct K3Ctx {
  __amdgpu_buffer_rsrc_t rs, r2, ro;
  unsigned ldw, va, va3o, vl, sA, sB, sAo, sBo;
  unsigned ta[4];
};
template <int I>
DI void k3_issueA(const K3Ctx& c) {
  constexpr int slot = I < NPRE ? I : (I - NPRE) % NSLOT;
  if constexpr (I < 14) {
#pragma unroll
    for (int u = 0; u < 4; ++u) glds16(c.r2, c.ldw + ring_addr(slot) + u * 1024, c.va, c.sA + (32 * I + 8 * u) * 896);
  } else {
#pragma unroll
    for (int u = 0; u < 4; ++u) glds16(c.ro, c.ldw + ring_addr(slot) + u * 1024, u == 3 ? c.va3o : c.va, c.sAo + (28 * (I - 14) + 8 * u) * 896);
  }
}
template <int I>
DI void k3_issueB(const K3Ctx& c, BFrag& B) {
  if constexpr (I < 14) {
    BLD128_SC1(B.b[0], c.vl, c.rs, c.sB + I * 1024);
    BLD128_SC1(B.b[1], c.vl, c.rs, c.sB + I * 1024 + 56 * 1024);
    BLD128_SC1(B.b[2], c.vl, c.rs, c.sB + I * 1024 + 2 * 56 * 1024);
    BLD128_SC1(B.b[3], c.vl, c.rs, c.sB + I * 1024 + 3 * 56 * 1024);
  } else {
    BLD128_SC1(B.b[0], c.vl, c.rs, c.sBo + (I - 14) * 1024);
    BLD128_SC1(B.b[1], c.vl, c.rs, c.sBo + (I - 14) * 1024 + 16 * 1024);
    BLD128_SC1(B.b[2], c.vl, c.rs, c.sBo + (I - 14) * 1024 + 2 * 16 * 1024);
    BLD128_SC1(B.b[3], c.vl, c.rs, c.sBo + (I - 14) * 1024 + 3 * 16 * 1024);
  }
}
struct AFrag { u32x2 l[4], h[4]; };
#define A_TIE(A) "+v"(A.l[0]), "+v"(A.l[1]), "+v"(A.l[2]), "+v"(A.l[3]), "+v"(A.h[0]), "+v"(A.h[1]), "+v"(A.h[2]), "+v"(A.h[3])
template <int SLOT>
DI void k3_readA(const K3Ctx& c, AFrag& A) {
  constexpr int hi = SLOT < 3 ? L1_W : 3 * 16384, SO = ring_addr(SLOT) - hi;
  const unsigned t0 = c.ta[0] + hi, t1 = c.ta[1] + hi, t2 = c.ta[2] + hi, t3 = c.ta[3] + hi;
  DSRTR(A.l[0], t0, SO); DSRTR(A.h[0], t0, SO + 2048); DSRTR(A.l[1], t1, SO); DSRTR(A.h[1], t1, SO + 2048);
  DSRTR(A.l[2], t2, SO); DSRTR(A.h[2], t2, SO + 2048); DSRTR(A.l[3], t3, SO); DSRTR(A.h[3], t3, SO + 2048);
}
DI void k3_mma(f32x4 (&acc)[4][4], const AFrag& A, const BFrag& B) {
  SCHED0();
#pragma unroll
  for (int cc = 0; cc < 4; ++cc)
#pragma unroll
    for (int tk = 0; tk < 4; ++tk) acc[cc][tk] = mfma16(cat2(A.l[cc], A.h[cc]), asb(B.b[tk]), acc[cc][tk]);
  SCHED0();
}
template <int I>
DI void k3_step(const K3Ctx& c, f32x4 (&acc)[4][4], BFrag& B, AFrag (&AP)[NPRE > 0 ? NPRE : 1]) {
  if constexpr (I < NPRE) { if constexpr (I >= NEARLY) k3_issueA<I + NSLOT>(c); k3_mma(acc, AP[I], B); }
  else {
    AFrag A;
    k3_readA<(I - NPRE) % NSLOT>(c, A);
    asm volatile("s_waitcnt lgkmcnt(0)" : A_TIE(A) :: "memory");
    if constexpr (I + NSLOT < 18) k3_issueA<I + NSLOT>(c);
    k3_mma(acc, A, B);
  }
}
constexpr int k3_ops(int j) { return (j >= NEARLY && j + NSLOT < 18 ? 4 : 0) + (j + 3 < 18 ? 4 : 0); }
constexpr int k3_wait(int i) { return i == 0 ? 8 : i == 1 ? 4 + k3_ops(0) : k3_ops(i - 2) + k3_ops(i - 1) + ((i >= 9 && i <= 11) ? 1 : 0); }
constexpr int k3_wait_fl() { return k3_ops(9) + k3_ops(10) + (11 + NSLOT < 18 ? 4 : 0); }
template <int I> DI void k3_waitB(BFrag& B) { asm volatile("s_waitcnt vmcnt(%4)" : B_TIE(B) : "i"(k3_wait(I)) : "memory"); }
template <int OFS, int... Is> DI void k3_prefillA(const K3Ctx& c, std::integer_sequence<int, Is...>) { (k3_issueA<OFS + Is>(c), ...); }
template <int... Is> DI void k3_prereadA(const K3Ctx& c, AFrag (&AP)[NPRE > 0 ? NPRE : 1], std::integer_sequence<int, Is...>) {
  ((k3_readA<Is>(c, AP[Is]), ({ asm volatile("s_waitcnt lgkmcnt(0)" : A_TIE(AP[Is]) :: "memory"); })), ...);
}

DI float xmax16_32(float v) {
  auto a = __builtin_amdgcn_permlane16_swap(__float_as_uint(v), __float_as_uint(v), false, false);
  v = fmaxf(__uint_as_float(a[0]), __uint_as_float(a[1]));
  auto b = __builtin_amdgcn_permlane32_swap(__float_as_uint(v), __float_as_uint(v), false, false);
  return fmaxf(__uint_as_float(b[0]), __uint_as_float(b[1]));
}

__global__ __launch_bounds__(512, 2) void gated_mix_fused(const unsigned short* __restrict__ x, const unsigned short* __restrict__ wqkv,
                                                          const unsigned short* __restrict__ w1, const unsigned short* __restrict__ gw,
                                                          const unsigned short* __restrict__ w2, const unsigned short* __restrict__ wo,
                                                          char* __restrict__ ws, unsigned short* __restrict__ out) {
  extern __shared__ __attribute__((aligned(16))) char smem[];
  unsigned long long pc; asm volatile("s_getpc_b64 %0" : "=s"(pc));
  const int tid = threadIdx.x, lane = tid & 63;
  const int w = __builtin_amdgcn_readfirstlane(tid >> 6);
  const int xcd = blockIdx.x & 7, idx = blockIdx.x >> 3;
  const bool gemm_wg = idx < 28;
  ST_DECL
  STAMP(0);
  const unsigned lds0 = (unsigned)(uintptr_t)(lds_vptr)smem;
  const int i16 = lane & 15, g = lane >> 4;
  const unsigned vl = lane * 16;
  const __amdgpu_buffer_rsrc_t rs = mkrsrc(ws, WS_BYTES);
  const unsigned long long tag = (hw_dispatch_id() << 20) ^ (unsigned long long)(uintptr_t)__builtin_amdgcn_dispatch_ptr() ^ 0x9e3779b97f4a7c15ull;
  const unsigned tg = (unsigned)tag ^ (unsigned)(tag >> 32);
  const int tt = 2 * xcd + (idx >= 14 ? 1 : 0), j = idx >= 14 ? idx - 14 : idx;
  const int qb = idx >> 1, h = 2 * xcd + (idx & 1);

  if (__builtin_expect(w >= 4, 1)) {
    const int lw = w - 4;
    const __amdgpu_buffer_rsrc_t r2 = mkrsrc(w2, FF * D_ * 2), ro = mkrsrc(wo, D_ * D_ * 2);
    const int t64 = 4 * xcd + (gemm_wg ? idx / 7 : 0), nt = gemm_wg ? idx % 7 : 0;
    K3Ctx c;
    unsigned fq;
    auto late_setup = [&]() {
      SCHED0();
      c.rs = rs; c.r2 = r2; c.ro = ro; c.vl = vl;
      c.ldw = lds0 + lw * 4096;
      { const int r8 = lane >> 3;
        const unsigned vchunk = (unsigned)(((lane & 7) ^ (2 * ((r8 >> 1) & 3))) << 4) + nt * 128;
        c.va = r8 * 896 + vchunk;
        c.va3o = (r8 < 4 ? r8 : 3) * 896 + vchunk; }
      c.sA = (unsigned)(14 * lw) * 32 * 896;
      c.sB = OFF_H + (unsigned)((4 * t64) * 56 + 14 * lw) * 1024;
      c.sAo = (unsigned)(4 * lw) * 28 * 896;
      c.sBo = OFF_O + (unsigned)((4 * t64) * 16 + 4 * lw) * 1024;
      { const int trow = 4 * g + (i16 >> 2);
#pragma unroll
        for (int cc = 0; cc < 4; ++cc) c.ta[cc] = c.ldw + trow * 128 + ((cc ^ ((trow >> 1) & 3)) * 32) + 8 * (i16 & 3); }
      { const int hb = h >> 2;
        int k = lane < 3 ? lane : (lane < 6 ? lane - 3 : 0);
        int tq = qb - k; tq = tq < 0 ? 0 : tq;
        fq = 128 * (lane < 3 ? tq * 32 + F_QKV + 4 + hb : (lane < 6 ? tq * 32 + F_QKV + 8 + hb : (lane == 6 ? qb * 32 + F_QKV + hb : F_G + 2 * qb + (lane & 1)))); }
    };
    if (__builtin_expect(gemm_wg, 1)) {
      const bool has1 = j < 12;
      const __amdgpu_buffer_rsrc_t rx = mkrsrc(x, S_ * D_ * 2);
      const __amdgpu_buffer_rsrc_t rq = mkrsrc(wqkv, D_ * 3 * D_ * 2), r1 = mkrsrc(w1, D_ * FF * 2);
      unsigned vx[2], vq[2], v1[2];
#pragma unroll
      for (int u = 0; u < 2; ++u) {
        vx[u] = (lane >> 3) * 896 + (((lane & 7) ^ (((lane >> 4) + 4 * u) & 7)) << 4);
        const unsigned p = lane & 15, f = (lane >> 4) + 4 * u;
        const unsigned cc = ((((p >> 1) ^ f) << 1) | (p & 1)) << 4;
        vq[u] = has1 ? (lane >> 4) * 2688 + cc : 0x80000000u;
        v1[u] = (lane >> 4) * 3584 + cc;
      }
      const unsigned sx0 = (unsigned)(128 * tt + 32 * lw) * 896u;
      const unsigned sq0 = (unsigned)(896 * (j >> 2) + 224 * (j & 3)) + 16 * lw * 2688;
      const unsigned s10 = (unsigned)(256 * j) + 16 * lw * 3584;
      auto issue = [&](int s, int slot) {
        const unsigned wb = lds0 + L1_W + slot * 16384 + 4 * lw * 1024;
        if (s < 7) {
#pragma unroll
          for (int u = 0; u < 4; ++u) glds16(rx, lds0 + L1_X + s * 16384 + (4 * lw + u) * 1024, vx[u & 1], sx0 + s * 128 + u * (8 * 896));
#pragma unroll
          for (int u = 0; u < 4; ++u) glds16(rq, wb + u * 1024, vq[u >> 1], sq0 + (s * 64 + 4 * u) * 2688);
        } else {
#pragma unroll
          for (int u = 0; u < 4; ++u) glds16(r1, wb + u * 1024, v1[u >> 1], s10 + ((s - 7) * 64 + 4 * u) * 3584);
        }
      };
      issue(0, 0); issue(1, 1);
      STAMP(1);
      late_setup();
      WAITV(8); BAR();
      issue(2, 2);
      int slot = 0;
#pragma unroll 1
      for (int s = 0; s < 7; ++s) {
        if (s < 5) { WAITV(8); } else { WAITV(4); }
        BAR();
        issue(s + 3, slot);
        slot = slot == 2 ? 0 : slot + 1;
      }
      auto issue_x = [&](int q) {
#pragma unroll
        for (int u = 0; u < 4; ++u) glds16(r1, lds0 + L1_X + q * 16384 + (4 * lw + u) * 1024, v1[u >> 1], s10 + ((3 + q) * 64 + 4 * u) * 3584);
      };
      WAITV(4); BAR();
      issue_x(0); k3_issueA<0>(c);
      WAITV(8); BAR();
      issue_x(1); k3_issueA<1>(c);
      WAITV(12); BAR();
      issue_x(2); k3_issueA<2>(c);
      WAITV(12); BAR();
      issue_x(3);
      WAITV(8); BAR();
      k3_issueA<3>(c);
      WAITV(4); BAR();
      k3_issueA<4>(c);
      BAR();
      STAMP(2);
    } else {
      late_setup();
    }
    wait_tags(rs, fq, tag);
    STAMP(3);
#pragma unroll
    for (int u = 0; u < 6; ++u) {
      int i = 6 * lw + u;
      int U = 8 * qb - 16 + i; U = U < 0 ? 0 : U;
      glds16_sc1(rs, lds0 + LDS_K + i * 1024, vl, OFF_K + (h * 128 + U) * 1024);
    }
#pragma unroll
    for (int u = 0; u < 6; ++u) {
      int i = 6 * lw + u;
      int m = 4 * qb - 8 + (i >> 1); m = m < 0 ? 0 : m;
      glds16_sc1(rs, lds0 + LDS_V + i * 1024, vl, OFF_V + ((h * 64 + m) * 2 + (i & 1)) * 1024);
    }
#pragma unroll
    for (int u = 0; u < 2; ++u) glds16_sc1(rs, lds0 + LDS_Q + (2 * lw + u) * 1024, vl, OFF_Q + (h * 128 + 8 * qb + 2 * lw + u) * 1024);
    if (lane < 8) glds16_sc1(rs, lds0 + LDS_G + lw * 128, vl, OFF_G + (128 * qb + 32 * lw) * 4);
    if (!gemm_wg) {
      WAITV(0);
      lds_mark(lds0 + LDS_SYNC, lw, tg, lane);
      ST_FLUSH(0);
      return;
    }
    k3_issueA<5>(c);
    WAITV(4);
    lds_mark(lds0 + LDS_SYNC, lw, tg, lane);
    WAITV(0);
    STAMP(4);
    AFrag AP[NPRE];
    k3_prereadA(c, AP, std::make_integer_sequence<int, NPRE>());
    k3_prefillA<NPRE>(c, std::make_integer_sequence<int, NEARLY>());
    wait_tags(rs, (lane & 4 ? F_G + t64 : (unsigned)((t64 >> 1) * 32 + F_H + ((7 * lw) >> 1) + (lane & 3))) * 128, tag);
    STAMP(5);
    float omg[4];
    BFrag B0, B1, B2;
    { unsigned g3[4]; const unsigned vg = i16 * 4;
      BLD32_SC1(g3[0], vg, rs, OFF_G + t64 * 256); BLD32_SC1(g3[1], vg, rs, OFF_G + t64 * 256 + 64);
      BLD32_SC1(g3[2], vg, rs, OFF_G + t64 * 256 + 128); BLD32_SC1(g3[3], vg, rs, OFF_G + t64 * 256 + 192);
      k3_issueB<0>(c, B0); k3_issueB<1>(c, B1); k3_issueB<2>(c, B2);
      asm volatile("s_waitcnt vmcnt(8)" : "+v"(g3[0]), "+v"(g3[1]), "+v"(g3[2]), "+v"(g3[3]), B_TIE(B0) :: "memory");
#pragma unroll
      for (int tk = 0; tk < 4; ++tk) omg[tk] = rbf(1.f - __uint_as_float(g3[tk])); }
    f32x4 acc[4][4];
#pragma unroll
    for (int cc = 0; cc < 4; ++cc)
#pragma unroll
      for (int tk = 0; tk < 4; ++tk) acc[cc][tk] = f32x4{0.f, 0.f, 0.f, 0.f};
    k3_step<0>(c, acc, B0, AP); k3_issueB<3>(c, B0);
    k3_waitB<1>(B1); k3_step<1>(c, acc, B1, AP); k3_issueB<4>(c, B1);
    k3_waitB<2>(B2); k3_step<2>(c, acc, B2, AP); k3_issueB<5>(c, B2);
    k3_waitB<3>(B0); k3_step<3>(c, acc, B0, AP); k3_issueB<6>(c, B0);
    k3_waitB<4>(B1); k3_step<4>(c, acc, B1, AP); k3_issueB<7>(c, B1);
    k3_waitB<5>(B2); k3_step<5>(c, acc, B2, AP); k3_issueB<8>(c, B2);
    k3_waitB<6>(B0); k3_step<6>(c, acc, B0, AP); k3_issueB<9>(c, B0);
    k3_waitB<7>(B1); k3_step<7>(c, acc, B1, AP); k3_issueB<10>(c, B1);
    STAMP(6);
    k3_waitB<8>(B2); k3_step<8>(c, acc, B2, AP); k3_issueB<11>(c, B2);
    u32x2 fl;
    const unsigned vf = (F_O + (unsigned)((t64 >> 1) * 16 + 4 * lw + (lane & 3))) * 128;
    BLD64_SC1(fl, vf, rs, OFF_F);
    k3_waitB<9>(B0); k3_step<9>(c, acc, B0, AP); k3_issueB<12>(c, B0);
    k3_waitB<10>(B1); k3_step<10>(c, acc, B1, AP); k3_issueB<13>(c, B1);
    k3_waitB<11>(B2); k3_step<11>(c, acc, B2, AP);
    asm volatile("s_waitcnt vmcnt(%1)" : "+v"(fl) : "i"(k3_wait_fl()) : "memory");
    if (!__all(fl[0] == (unsigned)tag && fl[1] == (unsigned)(tag >> 32))) wait_tags(rs, vf, tag);
    STAMP(7);
    k3_issueB<14>(c, B2);
    k3_waitB<12>(B0); k3_step<12>(c, acc, B0, AP); k3_issueB<15>(c, B0);
    k3_waitB<13>(B1); k3_step<13>(c, acc, B1, AP); k3_issueB<16>(c, B1);
#pragma unroll
    for (int tk = 0; tk < 4; ++tk)
#pragma unroll
      for (int cc = 0; cc < 4; ++cc) acc[cc][tk] *= omg[tk];
    k3_waitB<14>(B2); k3_step<14>(c, acc, B2, AP); k3_issueB<17>(c, B2);
    k3_waitB<15>(B0); k3_step<15>(c, acc, B0, AP);
    k3_waitB<16>(B1); k3_step<16>(c, acc, B1, AP);
    k3_waitB<17>(B2); k3_step<17>(c, acc, B2, AP);
    STAMP(8);
    BAR();
#pragma unroll
    for (int cc = 0; cc < 4; ++cc)
#pragma unroll
      for (int tk = 0; tk < 4; ++tk) *(lds_f32x4p)(uintptr_t)(lds0 + lw * 16384 + (tk * 4 + cc) * 1024 + vl) = acc[cc][tk];
    WAITL0();
    BAR();
    STAMP(9);
    const unsigned ob = lds0 + 65536 + lw * 2048;
#pragma unroll
    for (int cc = 0; cc < 4; ++cc) {
      f32x4 s = *(lds_f32x4p)(uintptr_t)(lds0 + (lw * 4 + cc) * 1024 + vl);
#pragma unroll
      for (int ww = 1; ww < 4; ++ww) s += *(lds_f32x4p)(uintptr_t)(lds0 + ww * 16384 + (lw * 4 + cc) * 1024 + vl);
      unsigned a = ob + i16 * 128 + (((2 * cc + (g >> 1)) ^ (i16 & 7)) << 4) + (g & 1) * 8;
      *(lds_u32x2p)(uintptr_t)a = u32x2{pk2(s[0], s[1]), pk2(s[2], s[3])};
    }
    WAITL0();
#pragma unroll
    for (int u = 0; u < 2; ++u) {
      int row = 8 * u + (lane >> 3), ch = lane & 7;
      u32x4 v = *(lds_u32x4p)(uintptr_t)(ob + row * 128 + ((ch ^ (row & 7)) << 4));
      *(u32x4*)((char*)out + (size_t)(64 * t64 + 16 * lw + row) * 896 + nt * 128 + ch * 16) = v;
    }
    STAMP(10);
    ST_FLUSH(0);
    return;
  }

  {
    const unsigned sink = lds0 + L1_X + 6 * 16384 + w * 1024;
    if (w == 0 && idx < CODE_KB) __builtin_amdgcn_global_load_lds((const unsigned*)(pc + idx * 1024 + vl), (lds_u32p)(uintptr_t)sink, 16, 0, 0);
    if (w == 1) {
      __builtin_amdgcn_raw_ptr_buffer_load_lds(rs, (lds_vptr)(uintptr_t)sink, 4, (unsigned)(lane < 17 ? lane : 16) << 20, 0, 0, 0);
      __builtin_amdgcn_raw_ptr_buffer_load_lds(mkrsrc(out, S_ * D_ * 2), (lds_vptr)(uintptr_t)(sink + 256), 4, (unsigned)(lane & 1) << 20, 0, 0, 0);
    }
    if (w == 2) {
      const unsigned pg = idx * 64 + lane;
      const unsigned short* base = pg < 448 ? x : (pg < 742 ? wqkv : (pg < 1134 ? w1 : (pg < 1526 ? w2 : wo)));
      const unsigned p0 = pg < 448 ? 0u : (pg < 742 ? 448u : (pg < 1134 ? 742u : (pg < 1526 ? 1134u : 1526u)));
      if (pg < 1624) __builtin_amdgcn_global_load_lds((const unsigned*)((const char*)base + (size_t)(pg - p0) * 4096), (lds_u32p)(uintptr_t)sink, 4, 0, 0);
    }
    {
      const __amdgpu_buffer_rsrc_t rq = mkrsrc(wqkv + 128 * 3 * D_, (D_ - 128) * 3 * D_ * 2), r1 = mkrsrc(w1, D_ * FF * 2);
      __builtin_amdgcn_s_sleep(BURST_DELAY);
      const unsigned pcs = blockIdx.x * 4 + w;
      if (pcs < 840) glds16(rq, sink, vl, pcs * 1024); else glds16(r1, sink, vl, (pcs - 840) * 1024);
      glds16(r1, sink, vl, (pcs + 184) * 1024);
      const __amdgpu_buffer_rsrc_t rw2 = mkrsrc(w2, FF * D_ * 2), rwo = mkrsrc(wo, D_ * D_ * 2);
      if (pcs < 360) glds16(r1, sink, vl, (pcs + 1208) * 1024); else glds16(rw2, sink, vl, (pcs - 360) * 1024);
      if (pcs < 904) glds16(rw2, sink, vl, (pcs + 664) * 1024); else glds16(rwo, sink, vl, (pcs - 904) * 1024);
      if (pcs < 272) glds16(rwo, sink, vl, (pcs + 120) * 1024);
    }
  }
  if (gemm_wg) {
    const int wm = w & 1, wn = w >> 1;
    const unsigned xrow = lds0 + L1_X + (64 * wm + i16) * 128;
    const unsigned xa0 = xrow + (((g) ^ (i16 >> 1)) << 4);
    const unsigned xa1 = xrow + (((4 + g) ^ (i16 >> 1)) << 4);
    const unsigned wrow = lds0 + L1_W + (8 * g + (i16 >> 2)) * 256;
    const unsigned fsw = (i16 >> 2) + 4 * (g & 1);
    const int t16 = 8 * tt + 4 * wm;
    f32x4 acc[4][4];
    unsigned wa[4];
    BAR();
    STAMP(1);
    if (j < 12) {
      const int type = j >> 2;
      const unsigned h0 = 4 * (j & 3) + 2 * wn;
#pragma unroll
      for (int c = 0; c < 4; ++c) {
        const unsigned bo = 56 * (2 * wn + (c >> 1)) + 32 * (c & 1) + 8 * (i16 & 3);
        wa[c] = wrow + (((bo >> 5) ^ fsw) << 5) + (bo & 31);
#pragma unroll
        for (int m = 0; m < 4; ++m) acc[c][m] = f32x4{0.f, 0.f, 0.f, 0.f};
      }
      if (type == 2) {
        p1_piece<true>(acc, xa0, xa1, wa, P1_LOC_FIRST, 2, false, ws, 0, tag, tid);
        STAMP(2);
        const unsigned m32 = 4 * tt + 2 * wm;
        const unsigned padv = i16 == 12 ? 0x3f803f80u : 0u;
#pragma unroll
        for (int c = 0; c < 4; ++c)
#pragma unroll
          for (int q = 0; q < 2; ++q) {
            u32x4 f = {pk2(acc[c][2 * q][0], acc[c][2 * q][1]), pk2(acc[c][2 * q][2], acc[c][2 * q][3]),
                       pk2(acc[c][2 * q + 1][0], acc[c][2 * q + 1][1]), pk2(acc[c][2 * q + 1][2], acc[c][2 * q + 1][3])};
            if ((c & 1) && i16 >= 12) f = u32x4{padv, padv, padv, padv};
            ST16(rs, OFF_V + (((h0 + (c >> 1)) * 64 + m32 + q) * 2 + (c & 1)) * 1024 + vl, f);
          }
      } else {
        p1_piece<false>(acc, xa0, xa1, wa, P1_LOC_FIRST, 2, false, ws, 0, tag, tid);
        STAMP(2);
        const unsigned qb0 = (type == 0 ? OFF_Q : OFF_K) + vl;
#pragma unroll
        for (int hh = 0; hh < 2; ++hh)
#pragma unroll
          for (int mt = 0; mt < 4; ++mt) {
            u32x4 f = {pk2(acc[2 * hh][mt][0], acc[2 * hh][mt][1]), pk2(acc[2 * hh][mt][2], acc[2 * hh][mt][3]),
                       pk2(acc[2 * hh + 1][mt][0], acc[2 * hh + 1][mt][1]), pk2(acc[2 * hh + 1][mt][2], acc[2 * hh + 1][mt][3])};
            if (g == 3) { f[2] = 0u; f[3] = 0u; }
            ST16(rs, qb0 + ((h0 + hh) * 128 + t16 + mt) * 1024, f);
          }
      }
    } else {
#pragma unroll 1
      for (int s = 0; s < 7; ++s) { if (s == 2) WAITV(0); BAR(); }
    }
    STAMP(3);
#pragma unroll
    for (int c = 0; c < 4; ++c) {
      wa[c] = wrow + (((4 * wn + c) ^ fsw) << 5) + 8 * (i16 & 3);
#pragma unroll
      for (int m = 0; m < 4; ++m) acc[c][m] = f32x4{0.f, 0.f, 0.f, 0.f};
    }
    p1_piece<false>(acc, xa0, xa1, wa, P1_LOC_SECOND, PUB_AT, j < 12, ws, tt * 32 + F_QKV + j, tag, tid);
    STAMP(4);
#pragma unroll
    for (int mt = 0; mt < 4; ++mt)
#pragma unroll
      for (int q = 0; q < 2; ++q) {
        u32x4 f = {relu2(pk2(acc[2 * q][mt][0], acc[2 * q][mt][1])), relu2(pk2(acc[2 * q][mt][2], acc[2 * q][mt][3])),
                   relu2(pk2(acc[2 * q + 1][mt][0], acc[2 * q + 1][mt][1])), relu2(pk2(acc[2 * q + 1][mt][2], acc[2 * q + 1][mt][3]))};
        ST16(rs, OFF_H + ((t16 + mt) * 56 + 4 * j + 2 * wn + q) * 1024 + vl, f);
      }
    STAMP(5);
    WAITV(0);
    lds_mark(lds0 + LDS_SYNC + 16, w, tg, lane);
    if (w == 0) { lds_wait4(lds0 + LDS_SYNC + 16, tg, lane); if (lane == 0) set_flag(ws, tt * 32 + F_H + j, tag); }
    STAMP(6);
  } else {
    const int gb = xcd * 4 + (idx - 28);
    const int t0 = gb * 64 + w * 16;
    u32x4 gv = {0, 0, 0, 0};
    if (lane < 56) gv = *(const u32x4*)(gw + lane * 8);
    float mine = 0.f;
#pragma unroll 1
    for (int hf = 0; hf < 2; ++hf) {
      u32x4 xv[8];
#pragma unroll
      for (int t = 0; t < 8; ++t) {
        xv[t] = u32x4{0, 0, 0, 0};
        if (lane < 56) xv[t] = *(const u32x4*)(x + (size_t)(t0 + 8 * hf + t) * D_ + lane * 8);
      }
#pragma unroll
      for (int t = 0; t < 8; ++t) {
        float s = 0.f;
#pragma unroll
        for (int jj = 0; jj < 4; ++jj) {
          s += rbf(__uint_as_float(xv[t][jj] << 16) * __uint_as_float(gv[jj] << 16));
          s += rbf(__uint_as_float(xv[t][jj] & 0xffff0000u) * __uint_as_float(gv[jj] & 0xffff0000u));
        }
#pragma unroll
        for (int o = 32; o > 0; o >>= 1) s += __shfl_xor(s, o);
        if (lane == 8 * hf + t) mine = s;
      }
    }
    float z = rbf(mine);
    float gg = rbf(1.f / (1.f + __expf(-z)));
    if (lane < 16) __hip_atomic_store((__attribute__((address_space(1))) unsigned*)(ws + OFF_G) + t0 + lane, __float_as_uint(gg), __ATOMIC_RELAXED, __HIP_MEMORY_SCOPE_AGENT);
    WAITV(0);
    lds_mark(lds0 + LDS_SYNC + 16, w, tg, lane);
    if (w == 0) { lds_wait4(lds0 + LDS_SYNC + 16, tg, lane); if (lane == 0) set_flag(ws, F_G + gb, tag); }
  }

  {
    __builtin_amdgcn_s_setprio(2);
    lds_wait4(lds0 + LDS_SYNC, tg, lane);
    STAMP(7);
    const int T0 = 8 * qb + 2 * w;
    const float NEG = -1e30f;
    f32x4 mlo, mhi;
#pragma unroll
    for (int r = 0; r < 4; ++r) { mlo[r] = (4 * g + r > i16) ? 0.f : NEG; mhi[r] = (4 * g + r <= i16) ? 0.f : NEG; }
    const f32x4 z4 = {0.f, 0.f, 0.f, 0.f};
    f32x4 s0[17], s1[17];
    float gq0, gq1;
    { const unsigned ka = lds0 + LDS_K + (2 * w) * 1024 + vl;
      u32x4 qf0, qf1, kf[18];
      { const unsigned qa = lds0 + LDS_Q + (2 * w) * 1024 + vl, ga = lds0 + LDS_G + (32 * w + i16) * 4;
        DSR128(qf0, qa, 0); DSR128(qf1, qa, 1024);
        asm volatile("ds_read_b32 %0, %2\n\tds_read_b32 %1, %2 offset:64" : "=&v"(gq0), "=&v"(gq1) : "v"(ga)); }
      DSR128(kf[0], ka, 0); DSR128(kf[1], ka, 1024); DSR128(kf[2], ka, 2048); DSR128(kf[3], ka, 3072); DSR128(kf[4], ka, 4096); DSR128(kf[5], ka, 5120);
      DSR128(kf[6], ka, 6144); DSR128(kf[7], ka, 7168); DSR128(kf[8], ka, 8192); DSR128(kf[9], ka, 9216);
      asm volatile("s_waitcnt lgkmcnt(4)" : "+v"(qf0), "+v"(qf1), "+v"(gq0), "+v"(gq1), "+v"(kf[0]), "+v"(kf[1]), "+v"(kf[2]), "+v"(kf[3]), "+v"(kf[4]), "+v"(kf[5]));
      const bf16x8 q0 = asb(qf0), q1 = asb(qf1);
      DSR128(kf[10], ka, 10240); DSR128(kf[11], ka, 11264);
      DSR128(kf[12], ka, 12288); DSR128(kf[13], ka, 13312); DSR128(kf[14], ka, 14336); DSR128(kf[15], ka, 15360); DSR128(kf[16], ka, 16384); DSR128(kf[17], ka, 17408);
      SCHED0();
#pragma unroll
      for (int i = 0; i < 6; ++i) {
        s0[i] = mfma16(asb(kf[i]), q0, i == 0 ? mlo : z4);
        if (i > 0) s1[i - 1] = mfma16(asb(kf[i]), q1, i == 1 ? mlo : z4);
      }
      SCHED0();
      asm volatile("s_waitcnt lgkmcnt(6)" : "+v"(kf[6]), "+v"(kf[7]), "+v"(kf[8]), "+v"(kf[9]), "+v"(kf[10]), "+v"(kf[11]));
#pragma unroll
      for (int i = 6; i < 12; ++i) { s0[i] = mfma16(asb(kf[i]), q0, z4); s1[i - 1] = mfma16(asb(kf[i]), q1, z4); }
      SCHED0();
      asm volatile("s_waitcnt lgkmcnt(0)" : "+v"(kf[12]), "+v"(kf[13]), "+v"(kf[14]), "+v"(kf[15]), "+v"(kf[16]), "+v"(kf[17]));
#pragma unroll
      for (int i = 12; i < 18; ++i) {
        if (i < 17) s0[i] = mfma16(asb(kf[i]), q0, i == 16 ? mhi : z4);
        s1[i - 1] = mfma16(asb(kf[i]), q1, i == 17 ? mhi : z4);
      }
    }
    if (T0 < 16) {
      const int first = 16 - T0;
#pragma unroll
      for (int i = 0; i < 16; ++i) {
        if (i < first) s0[i] = f32x4{NEG, NEG, NEG, NEG};
        if (i + 1 < first) s1[i] = f32x4{NEG, NEG, NEG, NEG};
      }
    }
    float m0 = NEG, m1 = NEG;
#pragma unroll
    for (int i = 0; i < 17; ++i) {
      m0 = fmaxf(fmaxf(m0, s0[i][0]), s0[i][1]); m0 = fmaxf(fmaxf(m0, s0[i][2]), s0[i][3]);
      m1 = fmaxf(fmaxf(m1, s1[i][0]), s1[i][1]); m1 = fmaxf(fmaxf(m1, s1[i][2]), s1[i][3]);
    }
    m0 = xmax16_32(m0); m1 = xmax16_32(m1);
    const float cs = 0.18898223650461357f * 1.4426950408889634f;
    const float n0 = -m0 * cs, n1 = -m1 * cs;
    f32x4 o0[2] = {z4, z4}, o1[2] = {z4, z4};
    const char* vp = smem + LDS_V + (2 * w) * 1024 + lane * 16;
    auto pexp = [&](const f32x4& v, float n, unsigned& a, unsigned& b) {
      a = pk2(__builtin_amdgcn_exp2f(fmaf(v[0], cs, n)), __builtin_amdgcn_exp2f(fmaf(v[1], cs, n)));
      b = pk2(__builtin_amdgcn_exp2f(fmaf(v[2], cs, n)), __builtin_amdgcn_exp2f(fmaf(v[3], cs, n)));
    };
#pragma unroll
    for (int m = 0; m < 9; ++m) {
      unsigned a0, a1, a2 = 0u, a3 = 0u, b0 = 0u, b1 = 0u, b2, b3;
      pexp(s0[2 * m], n0, a0, a1);
      if (m < 8) pexp(s0[2 * m + 1], n0, a2, a3);
      if (m > 0) pexp(s1[2 * m - 1], n1, b0, b1);
      pexp(s1[2 * m], n1, b2, b3);
      const u32x4 pa = {a0, a1, a2, a3}, pb = {b0, b1, b2, b3};
#pragma unroll
      for (int dt = 0; dt < 2; ++dt) {
        bf16x8 vf = *(const bf16x8*)(vp + (2 * m + dt) * 1024);
        o0[dt] = mfma16(vf, asb(pa), o0[dt]);
        o1[dt] = mfma16(vf, asb(pb), o1[dt]);
      }
    }
    STAMP(8);
    float l0 = __shfl(o0[1][0], 48 + i16), l1 = __shfl(o1[1][0], 48 + i16);
    float f0 = gq0 / l0, f1 = gq1 / l1;
    u32x4 r0 = {pk2(o0[0][0] * f0, o0[0][1] * f0), pk2(o0[0][2] * f0, o0[0][3] * f0), pk2(o0[1][0] * f0, o0[1][1] * f0), pk2(o0[1][2] * f0, o0[1][3] * f0)};
    u32x4 r1 = {pk2(o1[0][0] * f1, o1[0][1] * f1), pk2(o1[0][2] * f1, o1[0][3] * f1), pk2(o1[1][0] * f1, o1[1][1] * f1), pk2(o1[1][2] * f1, o1[1][3] * f1)};
    if (g == 3) { r0[2] = r0[3] = 0u; r1[2] = r1[3] = 0u; }
    ST16(rs, OFF_O + (T0 * 16 + h) * 1024 + vl, r0);
    ST16(rs, OFF_O + ((T0 + 1) * 16 + h) * 1024 + vl, r1);
    WAITV(0);
    lds_mark(lds0 + LDS_SYNC + 32, w, tg, lane);
    if (w == 0) { lds_wait4(lds0 + LDS_SYNC + 32, tg, lane); if (lane == 0) set_flag(ws, F_O + qb * 16 + h, tag); }
    STAMP(9);
    ST_FLUSH(0);
  }
}

extern "C" void kernel_launch(void* const* d_in, const int* in_sizes, int n_in, void* d_out, int out_size, void* d_ws,
                              size_t ws_size, hipStream_t stream) {
  const unsigned short* x = (const unsigned short*)d_in[0];
  const unsigned short* wqkv = (const unsigned short*)d_in[1];
  const unsigned short* wo = (const unsigned short*)d_in[2];
  const unsigned short* w1 = (const unsigned short*)d_in[4];
  const unsigned short* w2 = (const unsigned short*)d_in[5];
  const unsigned short* gw = (const unsigned short*)d_in[6];
  (void)hipFuncSetAttribute((const void*)gated_mix_fused, hipFuncAttributeMaxDynamicSharedMemorySize, LDS_TOTAL);
  gated_mix_fused<<<256, 512, LDS_TOTAL, stream>>>(x, wqkv, w1, gw, w2, wo, (char*)d_ws, (unsigned short*)d_out);
}
```
